```python
import torch

def _softmax(x, axis=-1):
    return torch.softmax(x, dim=axis)
def _gelu(x):
    return torch.nn.functional.gelu(x, approximate="tanh")
def _silu(x):
    return torch.nn.functional.silu(x)
def _sigmoid(x):
    return torch.sigmoid(x)
def _layernorm(x, axis=-1, eps=1e-5):
    m = x.mean(dim=axis, keepdim=True)
    v = x.var(dim=axis, keepdim=True, unbiased=False)
    return (x - m) / torch.sqrt(v + eps)
def _scatter_add(tgt, idx, src, axis=0):
    return tgt.clone().index_add_(axis, idx, src)
def _segment_sum(src, seg, n):
    out = torch.zeros((n,) + src.shape[1:], dtype=src.dtype)
    return out.index_add_(0, seg, src)
def _pad(x, pw):
    flat = tuple(v for pair in reversed(pw) for v in pair)
    return torch.nn.functional.pad(x, flat)

kernel_name = 'concat_heads__mha_block__grouped_kv_mqa__prompt'

def setup_inputs():

    _w = (lambda a: (lambda t: t.to(torch.bfloat16) if t.is_floating_point() else t)(torch.as_tensor(a)))
    def _A_setup():
        B, S, D, H = 1, 2048, 640, 32
        return {
            "x": _w((lambda *s: torch.randn(*s, dtype=torch.bfloat16) * 0.2)(B, S, D)),
            "wqkv": _w((lambda *s: torch.randn(*s, dtype=torch.bfloat16) * 0.2)(D, 3 * D)),
            "wo": _w((lambda *s: torch.randn(*s, dtype=torch.bfloat16) * 0.05)(D, D)),
        }
    _A_in = _A_setup()

    def _B_setup():
        B, S, D, H = 1, 2048, 640, 32
        Dh = D // H
        return {
            "x": _w((lambda *s: torch.randn(*s, dtype=torch.bfloat16) * 0.05)(B, S, D)),
            "wq": _w((lambda *s: torch.randn(*s, dtype=torch.bfloat16) * 0.8)(D, H * Dh)),
            "wk": _w((lambda *s: torch.randn(*s, dtype=torch.bfloat16) * 0.8)(D, Dh)),
            "wv": _w((lambda *s: torch.randn(*s, dtype=torch.bfloat16) * 0.1)(D, Dh)),
            "wo": _w((lambda *s: torch.randn(*s, dtype=torch.bfloat16) * 0.1)(H * Dh, D)),
        }
    _B_in = _B_setup()

    return {
        "A_x": _A_in["x"],
        "A_wqkv": _A_in["wqkv"],
        "A_wo": _A_in["wo"],
        "B_x": _B_in["x"],
        "B_wq": _B_in["wq"],
        "B_wk": _B_in["wk"],
        "B_wv": _B_in["wv"],
        "B_wo": _B_in["wo"],
    }

def reference(A_x, A_wqkv, A_wo, B_x, B_wq, B_wk, B_wv, B_wo):
    def _A_ref(x, wqkv, wo):
        B, S, D = x.shape
        H = 32
        Dh = D // H
        qkv = (lambda x, *d: x.permute(*d))((x @ wqkv).reshape(B, S, 3, H, Dh), 2, 0, 3, 1, 4)
        q, k, v = qkv[0], qkv[1], qkv[2]
        scores = torch.einsum("bhqd,bhkd->bhqk", q, k) * (Dh ** -0.5)
        p = _softmax(scores, axis=-1)
        o = torch.einsum("bhqk,bhkd->bhqd", p, v)
        return (lambda x, *d: x.permute(*d))(o, 0, 2, 1, 3).reshape(B, S, D) @ wo

    def _B_ref(x, wq, wk, wv, wo):
        B, S, D = x.shape
        H = 32
        Dh = D // H
        q = (lambda x, *d: x.permute(*d))((x @ wq).reshape(B, S, H, Dh), 0, 2, 1, 3)
        k = x @ wk
        v = x @ wv
        scores = torch.einsum("bhqd,bkd->bhqk", q, k) * (Dh ** -0.5)
        p = _softmax(scores, axis=-1)
        o = torch.einsum("bhqk,bkd->bhqd", p, v)
        return (lambda x, *d: x.permute(*d))(o, 0, 2, 1, 3).reshape(B, S, H * Dh) @ wo

    a = _A_ref(A_x, A_wqkv, A_wo)
    b = _B_ref(B_x, B_wq, B_wk, B_wv, B_wo)
    return torch.concatenate([a, b], axis=-1)
```

```cpp
#include <hip/hip_runtime.h>
#include <stdint.h>

typedef short bf16x8 __attribute__((ext_vector_type(8)));
typedef short bf16x4 __attribute__((ext_vector_type(4)));
typedef float f32x2 __attribute__((ext_vector_type(2)));
typedef float f32x4 __attribute__((ext_vector_type(4)));
typedef float f32x16 __attribute__((ext_vector_type(16)));
typedef unsigned u32x2 __attribute__((ext_vector_type(2)));
typedef unsigned u32x4 __attribute__((ext_vector_type(4)));
typedef __bf16 bf16x2_t __attribute__((ext_vector_type(2)));
typedef short v4i16_t __attribute__((ext_vector_type(4)));
typedef __attribute__((address_space(3))) const char* lds_cptr;

#define DI __device__ __forceinline__

constexpr int SEQ = 2048, DM = 640, NH = 32, DH = 20;
constexpr float QSCALE = 0.22360679774997896f * 1.4426950408889634f;

constexpr size_t QF_OFF = 0, QF_HEAD = (size_t)SEQ * 40;
constexpr size_t KF_OFF = QF_OFF + 64 * QF_HEAD, KF_HEAD = 64 * 1280;
constexpr size_t VF_OFF = KF_OFF + 33 * KF_HEAD, VF_HEAD = 128 * 672;
constexpr size_t OF_OFF = VF_OFF + 33 * VF_HEAD + 2048, OF_BR = (size_t)SEQ * DM * 2;

DI unsigned cvtpk(float lo, float hi) { f32x2 v = {lo, hi}; bf16x2_t b = __builtin_convertvector(v, bf16x2_t); return __builtin_bit_cast(unsigned, b); }
DI unsigned rfl(unsigned x) { return __builtin_amdgcn_readfirstlane(x); }
DI const char* rfl_ptr(const char* p) { uint64_t u = (uint64_t)p; uint64_t lo = rfl((unsigned)u), hi = rfl((unsigned)(u >> 32)); return (const char*)(lo | (hi << 32)); }
DI void glds16(const char* sbase, unsigned voff, unsigned lds_dst) {
    asm volatile("s_mov_b32 m0, %2\n\ts_nop 0\n\tglobal_load_lds_dwordx4 %0, %1" :: "v"(voff), "s"(sbase), "s"(lds_dst) : "memory");
}
DI bf16x8 lds_b128(lds_cptr p) { return *(const __attribute__((address_space(3))) bf16x8*)p; }
DI bf16x4 lds_tr(lds_cptr p) { return __builtin_bit_cast(bf16x4, __builtin_amdgcn_ds_read_tr16_b64_v4i16((__attribute__((address_space(3))) v4i16_t*)p)); }
DI bf16x8 cat8(bf16x4 a, bf16x4 b) { return __builtin_shufflevector(a, b, 0, 1, 2, 3, 4, 5, 6, 7); }
#define MFMA16(a, b, c) __builtin_amdgcn_mfma_f32_16x16x32_bf16(a, b, c, 0, 0, 0)
#define BARRIER() asm volatile("s_waitcnt lgkmcnt(0)\n\ts_barrier" ::: "memory")

constexpr int NSTAGE = DM / 64;
DI unsigned x_src_off(int piece, int lane, int pitch) { const int row = piece * 8 + (lane >> 3), pos = lane & 7; return (unsigned)(row * pitch + ((pos ^ ((row >> 1) & 7)) << 4)); }
DI unsigned w_src_off(int piece, int lane, int pitch) {
    const int o = piece * 1024 + lane * 16, rho = o / 160, cb = o - rho * 160;
    const int k = (rho & ~12) | ((rho & 4) << 1) | ((rho & 8) >> 1);
    return (unsigned)(k * pitch + cb);
}

constexpr int K1_X = 0, K1_W = 16384, K1_W2 = K1_W + 20480, K1_SLOT = K1_W2 + 3072, K1_LDS = 4 * K1_SLOT;

template <int NP> DI void k1_loader(const char* xg, const char* wg, const char* w2g, int wpitch, int lw, int lane, unsigned lds0) {
    unsigned xo[4], wo[5], w2o = 0;
#pragma unroll
    for (int i = 0; i < 4; ++i) xo[i] = x_src_off(4 * lw + i, lane, DM * 2);
#pragma unroll
    for (int i = 0; i < 5; ++i) { const int p = 5 * lw + i, half = p / 10; wo[i] = w_src_off(p - half * 10, lane, wpitch) + half * 160; }
    const int p2 = lw < 3 ? lw : 2;
    if (NP == 10) { const int o = p2 * 1024 + lane * 16; w2o = (unsigned)(o < 2560 ? o : 2544); }
    auto issue = [&](int s) {
        const unsigned sl = lds0 + (unsigned)(s & 3) * K1_SLOT;
        const char* xs = xg + (size_t)s * 128; const char* ws = wg + (size_t)s * 64 * wpitch;
#pragma unroll
        for (int i = 0; i < 4; ++i) glds16(xs, xo[i], sl + K1_X + (4 * lw + i) * 1024);
#pragma unroll
        for (int i = 0; i < 5; ++i) glds16(ws, wo[i], sl + K1_W + (5 * lw + i) * 1024);
        if (NP == 10) glds16(w2g + (size_t)s * 2560, w2o, sl + K1_W2 + p2 * 1024);
    };
    issue(0); issue(1); issue(2);
#pragma unroll 1
    for (int s = 0; s < NSTAGE; ++s) {
        if (s < NSTAGE - 2) { if (NP == 10) asm volatile("s_waitcnt vmcnt(20)" ::: "memory"); else asm volatile("s_waitcnt vmcnt(18)" ::: "memory"); }
        else if (s == NSTAGE - 2) { if (NP == 10) asm volatile("s_waitcnt vmcnt(10)" ::: "memory"); else asm volatile("s_waitcnt vmcnt(9)" ::: "memory"); }
        else asm volatile("s_waitcnt vmcnt(0)" ::: "memory");
        asm volatile("s_barrier" ::: "memory");
        if (s + 3 < NSTAGE) issue(s + 3);
    }
}

struct K1Frag { bf16x8 x[4]; bf16x8 w[5]; bf16x8 w2[2]; };

template <int MODE, int XTRA> DI void k1_consumer(char* ws, lds_cptr lds, int wid, int lane, int row0, int sec, int head0, int branch) {
    const int wm = wid >> 1, wn = wid & 1, m = lane & 15, g = lane >> 4, q = (lane & 15) >> 2, p = lane & 3;
    const unsigned xa0 = K1_X + (unsigned)((64 * wm + m) * 128 + ((g ^ ((m >> 1) & 7)) << 4));
    const unsigned wa = K1_W + (unsigned)(wn * 10240 + ((g >> 1) * 16 + (g & 1) * 4 + q) * 160 + p * 8);
    const unsigned w2a = K1_W2 + (unsigned)((8 * g + q) * 40 + p * 8);
    f32x4 acc[4][5], acc2[2][2];
#pragma unroll
    for (int i = 0; i < 4; ++i)
#pragma unroll
        for (int j = 0; j < 5; ++j) acc[i][j] = f32x4{0.f, 0.f, 0.f, 0.f};
#pragma unroll
    for (int i = 0; i < 2; ++i)
#pragma unroll
        for (int j = 0; j < 2; ++j) acc2[i][j] = f32x4{0.f, 0.f, 0.f, 0.f};
    auto load = [&](K1Frag& f, unsigned sl, int kk) {
        lds_cptr b = lds + sl;
#pragma unroll
        for (int i = 0; i < 4; ++i) f.x[i] = lds_b128(b + ((xa0 ^ (kk * 64)) + i * 2048));
#pragma unroll
        for (int j = 0; j < 5; ++j) f.w[j] = cat8(lds_tr(b + wa + kk * 5120 + j * 32), lds_tr(b + wa + kk * 5120 + 1280 + j * 32));
        if (XTRA) {
#pragma unroll
            for (int j = 0; j < 2; ++j) f.w2[j] = cat8(lds_tr(b + w2a + kk * 1280 + j * 32), lds_tr(b + w2a + kk * 1280 + 160 + j * 32));
        }
    };
    auto mma = [&](const K1Frag& f) {
#pragma unroll
        for (int i = 0; i < 4; ++i)
#pragma unroll
            for (int j = 0; j < 5; ++j) acc[i][j] = MODE ? MFMA16(f.x[i], f.w[j], acc[i][j]) : MFMA16(f.w[j], f.x[i], acc[i][j]);
        if (XTRA) {
#pragma unroll
            for (int i = 0; i < 2; ++i)
#pragma unroll
                for (int j = 0; j < 2; ++j) { const bf16x8 xx = wn ? f.x[2 + i] : f.x[i]; acc2[i][j] = XTRA == 2 ? MFMA16(xx, f.w2[j], acc2[i][j]) : MFMA16(f.w2[j], xx, acc2[i][j]); }
        }
    };
    K1Frag f0, f1;
    BARRIER();
    load(f0, 0, 0);
#pragma unroll 1
    for (int s = 0; s < NSTAGE; ++s) {
        const unsigned sl = (unsigned)(s & 3) * K1_SLOT;
        load(f1, sl, 1);
        mma(f0);
        if (s + 1 < NSTAGE) { BARRIER(); load(f0, (unsigned)((s + 1) & 3) * K1_SLOT, 0); }
        mma(f1);
    }
    if (MODE == 0) {
#pragma unroll
        for (int j = 0; j < 5; ++j) {
            const int col = 80 * wn + 16 * j + 4 * g, hl = col / 20, d0 = col - hl * 20, head = head0 + hl;
#pragma unroll
            for (int i = 0; i < 4; ++i) {
                const int row = row0 + 64 * wm + 16 * i + m;
                const f32x4 a = acc[i][j];
                if (sec == 0) {
                    u32x2 v = {cvtpk(a[0] * QSCALE, a[1] * QSCALE), cvtpk(a[2] * QSCALE, a[3] * QSCALE)};
                    *(u32x2*)(ws + QF_OFF + (size_t)(branch * 32 + head) * QF_HEAD + row * 40 + d0 * 2) = v;
                } else {
                    u32x2 v = {cvtpk(a[0], a[1]), cvtpk(a[2], a[3])};
                    const int key = row & 31, o = d0 < 16 ? (d0 >> 3) * 512 + key * 16 + (d0 & 4) * 2 : 1024 + key * 8;
                    *(u32x2*)(ws + KF_OFF + (size_t)head * KF_HEAD + (row >> 5) * 1280 + o) = v;
                }
            }
        }
    } else {
#pragma unroll
        for (int j = 0; j < 5; ++j) {
            const int col = 80 * wn + 16 * j + m, hl = col / 20, d = col - hl * 20, head = head0 + hl;
#pragma unroll
            for (int i = 0; i < 4; ++i) {
                const int blk = (row0 + 64 * wm + 16 * i) >> 4;
                const f32x4 a = acc[i][j];
                char* dst = ws + VF_OFF + (size_t)head * VF_HEAD + blk * 672 + (g & 1) * 336 + (g >> 1) * 8;
                *(u32x2*)(dst + d * 16) = u32x2{cvtpk(a[0], a[1]), cvtpk(a[2], a[3])};
                if (d == 0) *(u32x2*)(dst + 320) = u32x2{0x3F803F80u, 0x3F803F80u};
            }
        }
    }
    if (XTRA == 1) {
#pragma unroll
        for (int j = 0; j < 2; ++j) {
            const int d0 = 16 * j + 4 * g;
#pragma unroll
            for (int i = 0; i < 2; ++i) {
                const int row = row0 + 64 * wm + 32 * wn + 16 * i + m, key = row & 31;
                const f32x4 a = acc2[i][j];
                const int o = d0 < 16 ? (d0 >> 3) * 512 + key * 16 + (d0 & 4) * 2 : 1024 + key * 8;
                if (d0 < 20) *(u32x2*)(ws + KF_OFF + (size_t)32 * KF_HEAD + (row >> 5) * 1280 + o) = u32x2{cvtpk(a[0], a[1]), cvtpk(a[2], a[3])};
            }
        }
    }
    if (XTRA == 2) {
#pragma unroll
        for (int j = 0; j < 2; ++j) {
            const int d = 16 * j + m;
#pragma unroll
            for (int i = 0; i < 2; ++i) {
                const int blk = (row0 + 64 * wm + 32 * wn + 16 * i) >> 4;
                const f32x4 a = acc2[i][j];
                char* dst = ws + VF_OFF + (size_t)32 * VF_HEAD + blk * 672 + (g & 1) * 336 + (g >> 1) * 8;
                if (d < 20) *(u32x2*)(dst + d * 16) = u32x2{cvtpk(a[0], a[1]), cvtpk(a[2], a[3])};
                if (d == 0) *(u32x2*)(dst + 320) = u32x2{0x3F803F80u, 0x3F803F80u};
            }
        }
    }
}

__global__ __launch_bounds__(512, 2) void proj_kernel(const char* xA, const char* wqkv, const char* xB, const char* wq, const char* wk, const char* wv, char* ws) {
    extern __shared__ __attribute__((aligned(16))) char smem[];
    const int tid = threadIdx.x, lane = tid & 63, wid = (int)rfl(tid >> 6);
    const int xcd = blockIdx.x & 7, idx = blockIdx.x >> 3;
    const bool isB = xcd >= 6;
    const int rt = isB ? 8 * (xcd - 6) + (idx & 7) : 8 * (xcd / 3) + (idx & 7);
    const int ct = isB ? (idx >> 3) : 4 * (xcd % 3) + (idx >> 3);
    const int sec = isB ? 0 : ct >> 2, head0 = 8 * (ct & 3), row0 = rt * 128;
    const int xtra = isB && ct < 2 ? ct + 1 : 0;
    if (wid >= 4) {
        const char* xg = (isB ? xB : xA) + (size_t)row0 * (DM * 2);
        const int wpitch = isB ? DM * 2 : 3 * DM * 2;
        const char* wg = (isB ? wq : wqkv) + ct * 320;
        const char* w2g = xtra == 2 ? wv : wk;
        const unsigned lds0 = (unsigned)(uintptr_t)smem;
        if (xtra) k1_loader<10>(xg, wg, w2g, wpitch, wid - 4, lane, lds0); else k1_loader<9>(xg, wg, w2g, wpitch, wid - 4, lane, lds0);
    } else {
        lds_cptr lds = (lds_cptr)smem;
        if (xtra == 1) k1_consumer<0, 1>(ws, lds, wid, lane, row0, 0, head0, 1);
        else if (xtra == 2) k1_consumer<0, 2>(ws, lds, wid, lane, row0, 0, head0, 1);
        else if (sec == 2) k1_consumer<1, 0>(ws, lds, wid, lane, row0, sec, head0, 0);
        else k1_consumer<0, 0>(ws, lds, wid, lane, row0, sec, head0, isB ? 1 : 0);
    }
}

constexpr int K3_X = 0, K3_W = 16384, K3_SLOT = K3_W + 10240, K3_NS = 6, K3_LDS = K3_NS * K3_SLOT;

__global__ __launch_bounds__(512, 2) void outproj_kernel(const char* ws, const char* woA, const char* woB, char* out) {
    extern __shared__ __attribute__((aligned(16))) char smem[];
    const int tid = threadIdx.x, lane = tid & 63, wid = (int)rfl(tid >> 6);
    const int xcd = blockIdx.x & 7, idx = blockIdx.x >> 3;
    const int br = xcd >> 2, rt = 4 * (xcd & 3) + (idx & 3), ct = idx >> 2, row0 = rt * 128;
    if (wid >= 4) {
        const int lw = wid - 4;
        const char* xg = ws + OF_OFF + br * OF_BR + (size_t)row0 * (DM * 2);
        const char* wg = (br ? woB : woA) + ct * 160;
        const unsigned lds0 = (unsigned)(uintptr_t)smem;
        unsigned xo[4], wo[3]; int wp[3];
#pragma unroll
        for (int i = 0; i < 4; ++i) xo[i] = x_src_off(4 * lw + i, lane, DM * 2);
#pragma unroll
        for (int i = 0; i < 3; ++i) { int p = lw < 2 ? 3 * lw + i : 6 + 2 * (lw - 2) + (i < 2 ? i : 1); wp[i] = p; wo[i] = w_src_off(p, lane, DM * 2); }
        auto issue = [&](int s, int slot) {
            const unsigned sl = lds0 + (unsigned)slot * K3_SLOT;
            const char* xs = xg + (size_t)s * 128; const char* wsrc = wg + (size_t)s * 64 * (DM * 2);
#pragma unroll
            for (int i = 0; i < 4; ++i) glds16(xs, xo[i], sl + K3_X + (4 * lw + i) * 1024);
#pragma unroll
            for (int i = 0; i < 3; ++i) glds16(wsrc, wo[i], sl + K3_W + wp[i] * 1024);
        };
        issue(0, 0); issue(1, 1); issue(2, 2); issue(3, 3); issue(4, 4);
        asm volatile("s_waitcnt vmcnt(28)\n\ts_barrier" ::: "memory"); issue(5, 5);
        asm volatile("s_waitcnt vmcnt(28)\n\ts_barrier" ::: "memory"); issue(6, 0);
        asm volatile("s_waitcnt vmcnt(28)\n\ts_barrier" ::: "memory"); issue(7, 1);
        asm volatile("s_waitcnt vmcnt(28)\n\ts_barrier" ::: "memory"); issue(8, 2);
        asm volatile("s_waitcnt vmcnt(28)\n\ts_barrier" ::: "memory"); issue(9, 3);
        asm volatile("s_waitcnt vmcnt(28)\n\ts_barrier" ::: "memory");
        asm volatile("s_waitcnt vmcnt(21)\n\ts_barrier" ::: "memory");
        asm volatile("s_waitcnt vmcnt(14)\n\ts_barrier" ::: "memory");
        asm volatile("s_waitcnt vmcnt(7)\n\ts_barrier" ::: "memory");
        asm volatile("s_waitcnt vmcnt(0)\n\ts_barrier" ::: "memory");
    } else {
        lds_cptr lds = (lds_cptr)smem;
        const int m = lane & 15, g = lane >> 4, q = (lane & 15) >> 2, p = lane & 3;
        const unsigned xa0 = K3_X + (unsigned)((32 * wid + m) * 128 + ((g ^ ((m >> 1) & 7)) << 4));
        const unsigned wa = K3_W + (unsigned)(((g >> 1) * 16 + (g & 1) * 4 + q) * 160 + p * 8);
        f32x4 acc[2][5];
#pragma unroll
        for (int i = 0; i < 2; ++i)
#pragma unroll
            for (int j = 0; j < 5; ++j) acc[i][j] = f32x4{0.f, 0.f, 0.f, 0.f};
        bf16x8 x0[2], w0[5], x1[2], w1[5];
        auto load = [&](bf16x8* x, bf16x8* w, unsigned sl, int kk) {
            lds_cptr b = lds + sl;
#pragma unroll
            for (int i = 0; i < 2; ++i) x[i] = lds_b128(b + ((xa0 ^ (kk * 64)) + i * 2048));
#pragma unroll
            for (int j = 0; j < 5; ++j) w[j] = cat8(lds_tr(b + wa + kk * 5120 + j * 32), lds_tr(b + wa + kk * 5120 + 1280 + j * 32));
        };
        auto mma = [&](const bf16x8* x, const bf16x8* w) {
#pragma unroll
            for (int i = 0; i < 2; ++i)
#pragma unroll
                for (int j = 0; j < 5; ++j) acc[i][j] = MFMA16(w[j], x[i], acc[i][j]);
        };
        BARRIER();
        load(x0, w0, 0, 0);
        int slot = 0;
#pragma unroll 1
        for (int s = 0; s < NSTAGE; ++s) {
            load(x1, w1, (unsigned)slot * K3_SLOT, 1);
            mma(x0, w0);
            slot = slot == K3_NS - 1 ? 0 : slot + 1;
            if (s + 1 < NSTAGE) { BARRIER(); load(x0, w0, (unsigned)slot * K3_SLOT, 0); }
            mma(x1, w1);
        }
#pragma unroll
        for (int i = 0; i < 2; ++i) {
            const int row = row0 + 32 * wid + 16 * i + m;
#pragma unroll
            for (int j = 0; j < 5; ++j) {
                const f32x4 a = acc[i][j];
                *(u32x2*)(out + (size_t)row * 2560 + (br * 640 + ct * 80 + 16 * j + 4 * g) * 2) = u32x2{cvtpk(a[0], a[1]), cvtpk(a[2], a[3])};
            }
        }
    }
}

constexpr int A_K = 0, A_V = 10240, A_SLOT = A_V + 11264, A_LDS = 84 * 1024;

#define MFMA32(a, b, c) __builtin_amdgcn_mfma_f32_32x32x16_bf16(a, b, c, 0, 0, 0)
#define MFMA32R(a, b, c) __builtin_amdgcn_mfma_f32_32x32x8bf16_1k(a, b, c, 0, 0, 0)

DI void attn_exact(const char* kf, const char* vf, bf16x8 qm, bf16x4 qr, int lane, f32x16& o) {
    const int r32 = lane & 31, h = lane >> 5;
    float mrun = -3.0e38f;
#pragma unroll
    for (int r = 0; r < 16; ++r) o[r] = 0.f;
#pragma unroll 1
    for (int t = 0; t < 64; ++t) {
        const bf16x8 km = *(const bf16x8*)(kf + t * 1280 + lane * 16);
        const bf16x4 kr = *(const bf16x4*)(kf + t * 1280 + 1024 + r32 * 8);
        const int vrow = r32 < 21 ? r32 : 20;
        const bf16x8 v0 = *(const bf16x8*)(vf + (2 * t) * 672 + h * 336 + vrow * 16);
        const bf16x8 v1 = *(const bf16x8*)(vf + (2 * t + 1) * 672 + h * 336 + vrow * 16);
        f32x16 s;
#pragma unroll
        for (int r = 0; r < 16; ++r) s[r] = 0.f;
        s = MFMA32(km, qm, s);
        s = MFMA32R(kr, qr, s);
        float mx = s[0];
#pragma unroll
        for (int r = 1; r < 16; ++r) mx = fmaxf(mx, s[r]);
        { auto rr = __builtin_amdgcn_permlane32_swap(__float_as_uint(mx), __float_as_uint(mx), false, false); mx = fmaxf(__uint_as_float(rr[0]), __uint_as_float(rr[1])); }
        const float mnew = fmaxf(mrun, mx), alpha = __builtin_amdgcn_exp2f(mrun - mnew);
        mrun = mnew;
        u32x4 p0, p1;
#pragma unroll
        for (int i = 0; i < 4; ++i) {
            p0[i] = cvtpk(__builtin_amdgcn_exp2f(s[2 * i] - mnew), __builtin_amdgcn_exp2f(s[2 * i + 1] - mnew));
            p1[i] = cvtpk(__builtin_amdgcn_exp2f(s[8 + 2 * i] - mnew), __builtin_amdgcn_exp2f(s[8 + 2 * i + 1] - mnew));
        }
#pragma unroll
        for (int r = 0; r < 16; ++r) o[r] *= alpha;
        o = MFMA32(v0, __builtin_bit_cast(bf16x8, p0), o);
        o = MFMA32(v1, __builtin_bit_cast(bf16x8, p1), o);
    }
}

DI float row_sum_of(const f32x16& o) {
    auto rr = __builtin_amdgcn_permlane32_swap(__float_as_uint(o[8]), __float_as_uint(o[8]), false, false);
    return __uint_as_float(rr[1]);
}
DI bool o_ok(const f32x16& o, float l, int h) {
    bool ok = l >= 0x1p-60f && l <= 0x1p60f;
#pragma unroll
    for (int r = 0; r < 8; ++r) ok = ok && (fabsf(o[r]) <= 3.0e38f);
    if (h == 0) {
#pragma unroll
        for (int r = 8; r < 12; ++r) ok = ok && (fabsf(o[r]) <= 3.0e38f);
    }
    return ok;
}
DI void store_o(char* orow, const f32x16& o, float l, int h) {
    const float inv = 1.0f / l;
    *(u32x2*)(orow + 8 * h) = u32x2{cvtpk(o[0] * inv, o[1] * inv), cvtpk(o[2] * inv, o[3] * inv)};
    *(u32x2*)(orow + 16 + 8 * h) = u32x2{cvtpk(o[4] * inv, o[5] * inv), cvtpk(o[6] * inv, o[7] * inv)};
    if (h == 0) *(u32x2*)(orow + 32) = u32x2{cvtpk(o[8] * inv, o[9] * inv), cvtpk(o[10] * inv, o[11] * inv)};
}

#define R_SA "v[64:79]"
#define R_SB "v[80:95]"
#define R_PL "v[100:103]"
#define R_PH "v[104:107]"
#define R_KM "v[108:111]"
#define R_KR "v[112:113]"
#define R_V0 "v[114:117]"
#define R_V1 "v[118:121]"
#define STR_(x) #x
#define STR(x) STR_(x)
#define MF16(d, a, b, c) "v_mfma_f32_32x32x16_bf16 " d ", " a ", " b ", " c "\n\t"
#define MF8(d, a, b, c) "v_mfma_f32_32x32x8_bf16 " d ", " a ", " b ", " c "\n\t"
#define CVT2(p0, p1) "v_cvt_pk_bf16_f32 v" STR(p0) ", v96, v97\n\tv_cvt_pk_bf16_f32 v" STR(p1) ", v98, v99\n\t"
#define EXP4(s0, s1, s2, s3) "v_exp_f32 v96, v" STR(s0) "\n\tv_exp_f32 v97, v" STR(s1) "\n\tv_exp_f32 v98, v" STR(s2) "\n\tv_exp_f32 v99, v" STR(s3) "\n\t"
#define WAITL(n) "s_waitcnt lgkmcnt(" STR(n) ")\n\t"
#define RD128(d, a, off) "ds_read_b128 " d ", " a " offset:" STR(off) "\n\t"
#define RD64(d, a, off) "ds_read_b64 " d ", " a " offset:" STR(off) "\n\t"
#define TILE(PRE_K, KOFF, V1OFF, PRE_V0, V0OFF) \
    MF16(R_SB, R_KM, "%[qbm]", "0") PRE_K RD128(R_KM, "%[vk]", KOFF) CVT2(106, 107) EXP4(64, 65, 66, 67) \
    MF8(R_SB, R_KR, "%[qbr]", R_SB) RD64(R_KR, "%[vkr]", KOFF) CVT2(100, 101) EXP4(68, 69, 70, 71) \
    MF16("%[ob]", R_V1, R_PH, "%[ob]") RD128(R_V1, "%[vv]", V1OFF) CVT2(102, 103) EXP4(72, 73, 74, 75) \
    WAITL(3) MF16("%[oa]", R_V0, R_PL, "%[oa]") CVT2(104, 105) EXP4(76, 77, 78, 79) \
    WAITL(2) MF16(R_SA, R_KM, "%[qam]", "0") CVT2(106, 107) EXP4(80, 81, 82, 83) \
    WAITL(1) MF8(R_SA, R_KR, "%[qar]", R_SA) CVT2(100, 101) EXP4(84, 85, 86, 87) \
    WAITL(0) MF16("%[oa]", R_V1, R_PH, "%[oa]") CVT2(102, 103) EXP4(88, 89, 90, 91) \
    MF16("%[ob]", R_V0, R_PL, "%[ob]") PRE_V0 RD128(R_V0, "%[vv]", V0OFF) CVT2(104, 105) EXP4(92, 93, 94, 95)

#define DMA3(SLOTREG) \
    "v_add_u32 %[vd0], %[st0], %[vd0]\n\tv_add_u32 %[vd1], %[st1], %[vd1]\n\tv_add_u32 %[vd2], %[st2], %[vd2]\n\t" \
    "s_add_u32 m0, %[d0], " SLOTREG "\n\ts_nop 0\n\tglobal_load_lds_dwordx4 %[vd0], %[s0]\n\t" \
    "s_add_u32 m0, %[d1], " SLOTREG "\n\ts_nop 0\n\tglobal_load_lds_dwordx4 %[vd1], %[s1]\n\t" \
    "s_cmp_ge_u32 %[wid], 5\n\ts_cbranch_scc1 5f\n\t" \
    "s_add_u32 m0, %[d2], " SLOTREG "\n\ts_nop 0\n\tglobal_load_lds_dwordx4 %[vd2], %[s2]\n\t" \
    "5:\n\t"

__global__ __launch_bounds__(512, 2) void attn_kernel(char* ws) {
    extern __shared__ __attribute__((aligned(16))) char smem[];
    const int tid = threadIdx.x, lane = tid & 63, wid = (int)rfl(tid >> 6);
    const int xcd = blockIdx.x & 7, idx = blockIdx.x >> 3;
    const int bh = xcd + 8 * (idx >> 2), qblk = idx & 3;
    const int branch = (bh >> 3) & 1, head = (bh & 7) + 8 * (bh >> 4);
    const int kvh = branch ? 32 : head;
    const int r32 = lane & 31, h = lane >> 5;
    const char* kf = ws + KF_OFF + (size_t)kvh * KF_HEAD;
    const char* vf = ws + VF_OFF + (size_t)kvh * VF_HEAD;
    const unsigned lds0 = (unsigned)(uintptr_t)smem;
    const char* src[3]; unsigned dst[3], step[3];
#pragma unroll
    for (int i = 0; i < 3; ++i) {
        const int p = wid + 8 * i;
        const bool isK = p < 10;
        src[i] = rfl_ptr(isK ? kf + p * 1024 : vf + (p - 10) * 1024);
        dst[i] = rfl(lds0 + (isK ? A_K + p * 1024 : A_V + (p - 10) * 1024));
        step[i] = rfl(isK ? 10240u : 10752u);
    }
    unsigned vd0 = lane * 16, vd1 = lane * 16, vd2 = lane * 16;
    glds16(src[0], vd0, dst[0]); glds16(src[1], vd1, dst[1]);
    if (wid < 5) glds16(src[2], vd2, dst[2]);
    const int qrow0 = qblk * 512 + wid * 64;
    const char* qa = ws + QF_OFF + (size_t)(branch * 32 + head) * QF_HEAD + (size_t)(qrow0 + r32) * 40;
    const bf16x8 qam = *(const bf16x8*)(qa + 16 * h), qbm = *(const bf16x8*)(qa + 32 * 40 + 16 * h);
    bf16x4 qar = *(const bf16x4*)(qa + 32), qbr = *(const bf16x4*)(qa + 32 * 40 + 32);
    if (h) { qar = bf16x4{0, 0, 0, 0}; qbr = bf16x4{0, 0, 0, 0}; }
    f32x16 oa, ob;
#pragma unroll
    for (int r = 0; r < 16; ++r) { oa[r] = 0.f; ob[r] = 0.f; }
    unsigned vk = lds0 + A_K + lane * 16, vkr = lds0 + A_K + 1024 + r32 * 8, vv = lds0 + A_V + h * 336 + r32 * 16;
    asm volatile(
        "s_mov_b32 s44, 21504\n\t"
        "s_waitcnt vmcnt(0)\n\ts_barrier\n\t"
        DMA3("s44")
        RD128(R_KM, "%[vk]", 0) RD64(R_KR, "%[vkr]", 0) RD128(R_V0, "%[vv]", 0)
        "v_mov_b32 v96, 0\n\tv_mov_b32 v97, 0\n\tv_mov_b32 v98, 0\n\tv_mov_b32 v99, 0\n\t"
        "v_mov_b32 v104, 0\n\tv_mov_b32 v105, 0\n\t"
        "v_mov_b32 v118, 0\n\tv_mov_b32 v119, 0\n\tv_mov_b32 v120, 0\n\tv_mov_b32 v121, 0\n\t"
        "s_mov_b32 s40, 0\n\t"
        "s_mov_b32 s41, 0\n\t"
        WAITL(0)
        MF16(R_SA, R_KM, "%[qam]", "0") MF8(R_SA, R_KR, "%[qar]", R_SA)
        "s_nop 15\n\ts_nop 3\n\t"
        "2:\n\t"
        "s_add_u32 s42, s41, 21504\n\ts_cmp_eq_u32 s42, 64512\n\ts_cselect_b32 s42, 0, s42\n\t"
        "s_sub_u32 s43, s42, s41\n\t"
        "s_add_u32 s44, s42, 21504\n\ts_cmp_eq_u32 s44, 64512\n\ts_cselect_b32 s44, 0, s44\n\t"
        TILE("", 1280, 672, "", 1344)
        TILE("", 2560, 2016, "", 2688)
        TILE("", 3840, 3360, "", 4032)
        TILE("", 5120, 4704, "", 5376)
        TILE("", 6400, 6048, "", 6720)
        TILE("", 7680, 7392, "", 8064)
        TILE("", 8960, 8736, "", 9408)
        TILE(
            "s_waitcnt vmcnt(0)\n\ts_barrier\n\t"
            "v_add_u32 %[vk], s43, %[vk]\n\tv_add_u32 %[vkr], s43, %[vkr]\n\t"
            "s_cmp_ge_u32 s40, 6\n\ts_cbranch_scc1 3f\n\t"
            DMA3("s44")
            "3:\n\t",
            0, 10080, "v_add_u32 %[vv], s43, %[vv]\n\t", 0)
        "s_mov_b32 s41, s42\n\t"
        "s_add_u32 s40, s40, 1\n\t"
        "s_cmp_lt_u32 s40, 8\n\ts_cbranch_scc1 2b\n\t"
        "s_nop 1\n\t" CVT2(106, 107) "s_nop 1\n\t"
        MF16("%[ob]", R_V1, R_PH, "%[ob]")
        "s_waitcnt lgkmcnt(0)\n\ts_nop 15\n\ts_nop 3\n\t"
        : [oa] "+v"(oa), [ob] "+v"(ob), [vk] "+v"(vk), [vkr] "+v"(vkr), [vv] "+v"(vv), [vd0] "+v"(vd0), [vd1] "+v"(vd1), [vd2] "+v"(vd2)
        : [qam] "v"(qam), [qbm] "v"(qbm), [qar] "v"(qar), [qbr] "v"(qbr),
          [s0] "s"(src[0]), [s1] "s"(src[1]), [s2] "s"(src[2]), [d0] "s"(dst[0]), [d1] "s"(dst[1]), [d2] "s"(dst[2]), [wid] "s"(wid),
          [st0] "s"(step[0]), [st1] "s"(step[1]), [st2] "s"(step[2])
        : "memory", "scc", "s40", "s41", "s42", "s43", "s44",
          "v64", "v65", "v66", "v67", "v68", "v69", "v70", "v71", "v72", "v73", "v74", "v75", "v76", "v77", "v78", "v79",
          "v80", "v81", "v82", "v83", "v84", "v85", "v86", "v87", "v88", "v89", "v90", "v91", "v92", "v93", "v94", "v95",
          "v96", "v97", "v98", "v99", "v100", "v101", "v102", "v103", "v104", "v105", "v106", "v107", "v108", "v109", "v110", "v111",
          "v112", "v113", "v114", "v115", "v116", "v117", "v118", "v119", "v120", "v121");
    float la = row_sum_of(oa), lb = row_sum_of(ob);
    const bool ok = o_ok(oa, la, h) && o_ok(ob, lb, h);
    if (__builtin_expect(!__all(ok), 0)) {
        attn_exact(kf, vf, qam, qar, lane, oa); la = row_sum_of(oa);
        attn_exact(kf, vf, qbm, qbr, lane, ob); lb = row_sum_of(ob);
    }
    char* orow = ws + OF_OFF + branch * OF_BR + (size_t)(qrow0 + r32) * (DM * 2) + head * 40;
    store_o(orow, oa, la, h);
    store_o(orow + 32 * (DM * 2), ob, lb, h);
}

extern "C" void kernel_launch(void* const* d_in, const int* in_sizes, int n_in, void* d_out, int out_size, void* d_ws, size_t ws_size, hipStream_t stream) {
    (void)in_sizes; (void)n_in; (void)out_size; (void)ws_size;
    const char* xA = (const char*)d_in[0]; const char* wqkv = (const char*)d_in[1]; const char* woA = (const char*)d_in[2];
    const char* xB = (const char*)d_in[3]; const char* wq = (const char*)d_in[4]; const char* wk = (const char*)d_in[5]; const char* wv = (const char*)d_in[6]; const char* woB = (const char*)d_in[7];
    char* ws = (char*)d_ws;
    (void)hipFuncSetAttribute((const void*)proj_kernel, hipFuncAttributeMaxDynamicSharedMemorySize, K1_LDS);
    (void)hipFuncSetAttribute((const void*)attn_kernel, hipFuncAttributeMaxDynamicSharedMemorySize, A_LDS);
    (void)hipFuncSetAttribute((const void*)outproj_kernel, hipFuncAttributeMaxDynamicSharedMemorySize, K3_LDS);
    proj_kernel<<<256, 512, K1_LDS, stream>>>(xA, wqkv, xB, wq, wk, wv, ws);
    attn_kernel<<<256, 512, A_LDS, stream>>>(ws);
    outproj_kernel<<<256, 512, K3_LDS, stream>>>(ws, woA, woB, (char*)d_out);
}
```

```cpp
#include <hip/hip_runtime.h>
#include <stdint.h>

typedef short bf16x8 __attribute__((ext_vector_type(8)));
typedef short bf16x4 __attribute__((ext_vector_type(4)));
typedef float f32x2 __attribute__((ext_vector_type(2)));
typedef float f32x4 __attribute__((ext_vector_type(4)));
typedef float f32x16 __attribute__((ext_vector_type(16)));
typedef unsigned u32x2 __attribute__((ext_vector_type(2)));
typedef unsigned u32x4 __attribute__((ext_vector_type(4)));
typedef __bf16 bf16x2_t __attribute__((ext_vector_type(2)));
typedef short v4i16_t __attribute__((ext_vector_type(4)));
typedef __attribute__((address_space(3))) const char* lds_cptr;
typedef __attribute__((address_space(3))) char* lds_ptr;

#define DI __device__ __forceinline__

constexpr int SEQ = 2048, DM = 640;
constexpr float QSCALE = 0.22360679774997896f * 1.4426950408889634f;

constexpr unsigned HEAD40 = SEQ * 40;
constexpr unsigned QF_OFF = 0, KF_OFF = QF_OFF + 64 * HEAD40, VF_HEAD = 128 * 672, VF_OFF = KF_OFF + 33 * HEAD40, OF_OFF = VF_OFF + 33 * VF_HEAD + 2048;

DI unsigned cvtpk(float lo, float hi) { f32x2 v = {lo, hi}; bf16x2_t b = __builtin_convertvector(v, bf16x2_t); return __builtin_bit_cast(unsigned, b); }
DI u32x2 pack4(f32x4 a) { return u32x2{cvtpk(a[0], a[1]), cvtpk(a[2], a[3])}; }
DI unsigned rfl(unsigned x) { return __builtin_amdgcn_readfirstlane(x); }
DI void glds16(const char* sbase, unsigned voff, unsigned lds_dst) {
    asm volatile("s_mov_b32 m0, %2\n\ts_nop 0\n\tglobal_load_lds_dwordx4 %0, %1" :: "v"(voff), "s"(sbase), "s"(lds_dst) : "memory");
}
template <int N> DI void wait_vm() { asm volatile("s_waitcnt vmcnt(%0)" :: "n"(N) : "memory"); }
DI bf16x8 lds_b128(lds_cptr p) { return *(const __attribute__((address_space(3))) bf16x8*)p; }
DI bf16x4 lds_b64(lds_cptr p) { return *(const __attribute__((address_space(3))) bf16x4*)p; }
DI bf16x4 lds_tr(lds_cptr p) { return __builtin_bit_cast(bf16x4, __builtin_amdgcn_ds_read_tr16_b64_v4i16((__attribute__((address_space(3))) v4i16_t*)p)); }
DI void lds_st64(lds_ptr p, u32x2 v) { *(__attribute__((address_space(3))) u32x2*)p = v; }
DI bf16x8 cat8(bf16x4 a, bf16x4 b) { return __builtin_shufflevector(a, b, 0, 1, 2, 3, 4, 5, 6, 7); }
DI int swap23(int r) { return (r & ~12) | ((r & 4) << 1) | ((r & 8) >> 1); }
#define MFMA16(a, b, c) __builtin_amdgcn_mfma_f32_16x16x32_bf16(a, b, c, 0, 0, 0)
#define BARRIER() do { asm volatile("" ::: "memory"); __builtin_amdgcn_s_waitcnt(0xC07F); __builtin_amdgcn_s_barrier(); asm volatile("" ::: "memory"); } while (0)
#define SBAR() __builtin_amdgcn_sched_barrier(0)
template <int NM, int NR> DI void interleave() {
#pragma unroll
    for (int i = 0; i < NR; ++i) { __builtin_amdgcn_sched_group_barrier(0x008, 1, 0); __builtin_amdgcn_sched_group_barrier(0x100, 1, 0); }
    __builtin_amdgcn_sched_group_barrier(0x008, NM - NR, 0);
    SBAR();
}
#define STAMP(kern, slot)
#define DIAG_DRAIN()

constexpr int NSTAGE = DM / 64;
constexpr int K1_X = 0, K1_W = 16384, K1_W2 = K1_W + 24576, K1_SLOT = K1_W2 + 3072, K1_LDS = 3 * K1_SLOT;

template <int NP> DI void k1_loader(const char* xg, const char* wg, const char* w2g, int wpitch, int lw, int lane, unsigned lds0) {
    unsigned xo[4], wo[6], w2o = 0;
#pragma unroll
    for (int i = 0; i < 4; ++i) { const int row = (4 * lw + i) * 8 + (lane >> 3), pos = lane & 7; xo[i] = (unsigned)(row * (DM * 2) + ((pos ^ ((row >> 1) & 7)) << 4)); }
#pragma unroll
    for (int i = 0; i < 6; ++i) {
        const int o = (6 * lw + i) * 1024 + lane * 16, rho = o / 384, b = o - rho * 384;
        wo[i] = (unsigned)(swap23(rho) * wpitch + (((b >> 5) ^ ((rho >> 1) & 3)) << 5) + (b & 16));
    }
    const int p2 = lw < 3 ? lw : 2;
    if (NP == 11) { const int o = p2 * 1024 + lane * 16; w2o = (unsigned)(o < 2560 ? o : 2544); }
    auto issue = [&](int s, int slot) {
        const unsigned sl = lds0 + (unsigned)slot * K1_SLOT;
        const char* xs = xg + (size_t)s * 128; const char* wsrc = wg + (size_t)s * 64 * wpitch;
#pragma unroll
        for (int i = 0; i < 4; ++i) glds16(xs, xo[i], sl + K1_X + (4 * lw + i) * 1024);
#pragma unroll
        for (int i = 0; i < 6; ++i) glds16(wsrc, wo[i], sl + K1_W + (6 * lw + i) * 1024);
        if (NP == 11) glds16(w2g + (size_t)s * 2560, w2o, sl + K1_W2 + p2 * 1024);
    };
    issue(0, 0); issue(1, 1);
    int nslot = 2;
#pragma unroll 1
    for (int s = 0; s < NSTAGE; ++s) {
        if (s < NSTAGE - 1) wait_vm<NP>(); else wait_vm<0>();
        asm volatile("s_barrier" ::: "memory");
        if (s + 2 < NSTAGE) { issue(s + 2, nslot); nslot = nslot == 2 ? 0 : nslot + 1; }
    }
}

struct K1Frag { bf16x8 x[4]; bf16x8 w[5]; bf16x8 w2[2]; };

template <int KIND, int XTRA> DI void k1_consumer(char* ws, char* smem, int wid, int lane, int shift32) {
    lds_cptr lds = (lds_cptr)smem;
    const int wm = wid >> 1, wn = wid & 1, m = lane & 15, g = lane >> 4, q = (lane & 15) >> 2, p = lane & 3;
    const unsigned xa0 = K1_X + (unsigned)((64 * wm + m) * 128 + ((g ^ ((m >> 1) & 7)) << 4));
    unsigned wa[5];
    { const int gx = ((g & 1) << 1) | (q >> 1);
#pragma unroll
      for (int j = 0; j < 5; ++j) wa[j] = K1_W + (unsigned)(((g >> 1) * 16 + (g & 1) * 4 + q) * 384 + (((shift32 + 5 * wn + j) ^ gx) << 5) + p * 8); }
    const unsigned w2a = K1_W2 + (unsigned)((8 * g + q) * 40 + p * 8);
    f32x4 acc[4][5], acc2[2][2];
#pragma unroll
    for (int i = 0; i < 4; ++i)
#pragma unroll
        for (int j = 0; j < 5; ++j) acc[i][j] = f32x4{0.f, 0.f, 0.f, 0.f};
#pragma unroll
    for (int i = 0; i < 2; ++i)
#pragma unroll
        for (int j = 0; j < 2; ++j) acc2[i][j] = f32x4{0.f, 0.f, 0.f, 0.f};
    auto load = [&](K1Frag& f, unsigned sl, int kk) {
        lds_cptr b = lds + sl;
#pragma unroll
        for (int i = 0; i < 4; ++i) f.x[i] = lds_b128(b + ((xa0 ^ (kk * 64)) + i * 2048));
#pragma unroll
        for (int j = 0; j < 5; ++j) f.w[j] = cat8(lds_tr(b + wa[j] + kk * 12288), lds_tr(b + wa[j] + kk * 12288 + 3072));
        if (XTRA) {
#pragma unroll
            for (int j = 0; j < 2; ++j) f.w2[j] = cat8(lds_tr(b + w2a + kk * 1280 + j * 32), lds_tr(b + w2a + kk * 1280 + 160 + j * 32));
        }
    };
    auto mma = [&](const K1Frag& f) {
#pragma unroll
        for (int i = 0; i < 4; ++i)
#pragma unroll
            for (int j = 0; j < 5; ++j) acc[i][j] = KIND == 2 ? MFMA16(f.x[i], f.w[j], acc[i][j]) : MFMA16(f.w[j], f.x[i], acc[i][j]);
        if (XTRA) {
#pragma unroll
            for (int i = 0; i < 2; ++i)
#pragma unroll
                for (int j = 0; j < 2; ++j) { const bf16x8 xx = wn ? f.x[2 + i] : f.x[i]; acc2[i][j] = XTRA == 2 ? MFMA16(xx, f.w2[j], acc2[i][j]) : MFMA16(f.w2[j], xx, acc2[i][j]); }
        }
    };
    K1Frag f0, f1;
    STAMP(1, 0);
    BARRIER();
    STAMP(1, 1);
    load(f0, 0, 0);
    int slot = 0;
    constexpr int NM = XTRA ? 24 : 20, NR = XTRA ? 18 : 14;
    SBAR();
#pragma unroll 1
    for (int s = 0; s < NSTAGE - 1; ++s) {
        load(f1, (unsigned)slot * K1_SLOT, 1);
        mma(f0);
        interleave<NM, NR>();
        slot = slot == 2 ? 0 : slot + 1;
        BARRIER();
        load(f0, (unsigned)slot * K1_SLOT, 0);
        mma(f1);
        interleave<NM, NR>();
    }
    load(f1, (unsigned)slot * K1_SLOT, 1);
    mma(f0);
    interleave<NM, NR>();
    mma(f1);
    STAMP(1, 2);
    BARRIER();
    STAMP(1, 3);
    lds_ptr st = (lds_ptr)smem;
    constexpr int CH = KIND == 2 ? 5376 : 5120;
    if (KIND != 2) {
#pragma unroll
        for (int j = 0; j < 5; ++j) {
            const int col = 80 * wn + 16 * j + 4 * g, hl = col / 20, d0 = col - hl * 20;
#pragma unroll
            for (int i = 0; i < 4; ++i) {
                const int row = 64 * wm + 16 * i + m;
                if (KIND == 0) lds_st64(st + hl * CH + row * 40 + d0 * 2, pack4(acc[i][j] * QSCALE));
                else lds_st64(st + hl * CH + (row >> 5) * 1280 + (d0 < 16 ? (d0 >> 3) * 512 + (row & 31) * 16 + (d0 & 4) * 2 : 1024 + (row & 31) * 8), pack4(acc[i][j]));
            }
        }
    } else {
#pragma unroll
        for (int j = 0; j < 5; ++j) {
            const int col = 80 * wn + 16 * j + m, hl = col / 20, d = col - hl * 20;
#pragma unroll
            for (int i = 0; i < 4; ++i) {
                lds_ptr dst = st + hl * CH + (4 * wm + i) * 672 + (g & 1) * 336 + (g >> 1) * 8;
                lds_st64(dst + d * 16, pack4(acc[i][j]));
                if (d == 0) lds_st64(dst + 320, u32x2{0x3F803F80u, 0x3F803F80u});
            }
        }
    }
    if (XTRA == 1) {
#pragma unroll
        for (int j = 0; j < 2; ++j) {
            const int d0 = 16 * j + 4 * g;
#pragma unroll
            for (int i = 0; i < 2; ++i) {
                const int row = 64 * wm + 32 * wn + 16 * i + m;
                if (d0 < 20) lds_st64(st + 8 * CH + (row >> 5) * 1280 + (d0 < 16 ? (d0 >> 3) * 512 + (row & 31) * 16 + (d0 & 4) * 2 : 1024 + (row & 31) * 8), pack4(acc2[i][j]));
            }
        }
    }
    if (XTRA == 2) {
#pragma unroll
        for (int j = 0; j < 2; ++j) {
            const int d = 16 * j + m;
#pragma unroll
            for (int i = 0; i < 2; ++i) {
                lds_ptr dst = st + 8 * CH + (4 * wm + 2 * wn + i) * 672 + (g & 1) * 336 + (g >> 1) * 8;
                if (d < 20) lds_st64(dst + d * 16, pack4(acc2[i][j]));
                if (d == 0) lds_st64(dst + 320, u32x2{0x3F803F80u, 0x3F803F80u});
            }
        }
    }
    BARRIER();
    STAMP(1, 4);
}

template <int CH> DI void k1_copy_out(const char* smem, char* dst, unsigned stride, int tid) {
    lds_cptr lds = (lds_cptr)smem;
#pragma unroll
    for (int it = 0; it < (8 * CH + 8191) / 8192; ++it) {
        const int o = it * 8192 + tid * 16;
        if (o < 8 * CH) { const int c = o / CH, w = o - c * CH; *(bf16x8*)(dst + (size_t)c * stride + w) = lds_b128(lds + o); }
    }
}

__global__ __launch_bounds__(512, 2) void proj_kernel(const char* xA, const char* wqkv, const char* xB, const char* wq, const char* wk, const char* wv, char* ws) {
    extern __shared__ __attribute__((aligned(16))) char smem[];
    const int tid = threadIdx.x, lane = tid & 63, wid = (int)rfl(tid >> 6);
    const int xcd = blockIdx.x & 7, idx = blockIdx.x >> 3;
    const bool isB = xcd >= 6;
    const int rt = isB ? 8 * (xcd - 6) + (idx & 7) : 8 * (xcd / 3) + (idx & 7);
    const int ct = isB ? (idx >> 3) : 4 * (xcd % 3) + (idx >> 3);
    const int kind = isB ? 0 : ct >> 2, head0 = 8 * (ct & 3), row0 = rt * 128;
    const int xtra = isB && ct < 2 ? ct + 1 : 0;
    if (wid >= 4) {
        const char* xg = (isB ? xB : xA) + (size_t)row0 * (DM * 2);
        const int wpitch = isB ? DM * 2 : 3 * DM * 2;
        const char* wg = (isB ? wq : wqkv) + ((ct * 320) & ~127);
        const char* w2g = xtra == 2 ? wv : wk;
        const unsigned lds0 = (unsigned)(uintptr_t)smem;
        if (xtra) k1_loader<11>(xg, wg, w2g, wpitch, wid - 4, lane, lds0); else k1_loader<10>(xg, wg, w2g, wpitch, wid - 4, lane, lds0);
        asm volatile("s_barrier\n\ts_barrier" ::: "memory");
    } else {
        const int shift32 = (ct & 1) * 2;
        if (xtra == 1) k1_consumer<0, 1>(ws, smem, wid, lane, shift32);
        else if (xtra == 2) k1_consumer<0, 2>(ws, smem, wid, lane, shift32);
        else if (kind == 2) k1_consumer<2, 0>(ws, smem, wid, lane, shift32);
        else if (kind == 1) k1_consumer<1, 0>(ws, smem, wid, lane, shift32);
        else k1_consumer<0, 0>(ws, smem, wid, lane, shift32);
    }
    if (kind == 2) k1_copy_out<5376>(smem, ws + VF_OFF + (size_t)head0 * VF_HEAD + (row0 >> 4) * 672, VF_HEAD, tid);
    else if (kind == 1) k1_copy_out<5120>(smem, ws + KF_OFF + (size_t)head0 * HEAD40 + (row0 >> 5) * 1280, HEAD40, tid);
    else k1_copy_out<5120>(smem, ws + QF_OFF + (size_t)((isB ? 32 : 0) + head0) * HEAD40 + row0 * 40, HEAD40, tid);
    if (xtra && tid < 336) {
        const int nb = xtra == 1 ? 5120 : 5376;
        char* dst = xtra == 1 ? ws + KF_OFF + (size_t)32 * HEAD40 + (row0 >> 5) * 1280 : ws + VF_OFF + (size_t)32 * VF_HEAD + (row0 >> 4) * 672;
        if (tid * 16 < nb) *(bf16x8*)(dst + tid * 16) = lds_b128((lds_cptr)smem + 8 * 5120 + tid * 16);
    }
    if (wid < 4) { STAMP(1, 5); DIAG_DRAIN(); STAMP(1, 7); }
}

constexpr int K3_X = 0, K3_W = 32 * 3840, K3_LDS = K3_W + 4 * 8192;
constexpr int k3_total(int t) { int n = 14; for (int u = 0; u < t; ++u) n += (u <= 16 ? 2 : 0) + (u <= 10 ? 2 : 0); return n; }
constexpr int k3_allowed(int t) {
    int last = t <= 2 ? 10 + 2 * t : k3_total(t - 3) + 2;
    const int xg = t == 5 ? 3 : t == 10 ? 7 : t == 15 ? 10 : -1;
    if (xg >= 0) { const int e = k3_total(xg) + 4; if (e > last) last = e; }
    return k3_total(t) - last;
}
DI int k3_krow(int c, int e) { const int P = c / 5, r = c - 5 * P; return r < 4 ? (2 * P + (r >> 1)) * 20 + (r & 1) * 8 + e : (2 * P + (e >> 2)) * 20 + 16 + (e & 3); }

template <int T> DI void k3_load_steps(const char* xsrc, const char* wsrc, const unsigned (&wo)[5][2], int lw, int lane, unsigned lds0) {
    if constexpr (T < 20) {
        wait_vm<k3_allowed(T)>();
        asm volatile("s_barrier" ::: "memory");
        if constexpr (T + 3 < 20) {
            constexpr int t = T + 3;
#pragma unroll
            for (int i = 0; i < 2; ++i) glds16(wsrc + (size_t)(t / 5) * 160 * (DM * 2), wo[t % 5][i], lds0 + K3_W + (t & 3) * 8192 + (2 * lw + i) * 1024);
        }
        if constexpr (T <= 10) {
#pragma unroll
            for (int i = 0; i < 2; ++i) { const int pc = lw + 4 * (8 + 2 * T + i), o = pc * 1024 + lane * 16, hd = o / 3840; glds16(xsrc, (unsigned)(hd * HEAD40 + (o - hd * 3840)), lds0 + K3_X + pc * 1024); }
        }
        k3_load_steps<T + 1>(xsrc, wsrc, wo, lw, lane, lds0);
    }
}

__global__ __launch_bounds__(512, 2) void outproj_kernel(char* ws, const char* woA, const char* woB, char* out) {
    extern __shared__ __attribute__((aligned(16))) char smem[];
    const int tid = threadIdx.x, lane = tid & 63, wid = (int)rfl(tid >> 6);
    const int L = (blockIdx.x & 7) * 28 + (blockIdx.x >> 3);
    if (L >= 220) return;
    const int br = L / 110, rt = (L - br * 110) / 5, ct = L % 5, row0 = rt * 96 < SEQ - 96 ? rt * 96 : SEQ - 96;
    if (wid >= 4) {
        const int lw = wid - 4;
        const char* xsrc = ws + OF_OFF + (size_t)br * 32 * HEAD40 + row0 * 40;
        const char* wsrc = (br ? woB : woA) + ct * 256;
        const unsigned lds0 = (unsigned)(uintptr_t)smem;
        unsigned wo[5][2];
#pragma unroll
        for (int tt = 0; tt < 5; ++tt)
#pragma unroll
            for (int i = 0; i < 2; ++i) {
                const int rho = 4 * (2 * lw + i) + (lane >> 4), s16 = lane & 15, kap = swap23(rho);
                wo[tt][i] = (unsigned)(k3_krow(4 * tt + (kap >> 3), kap & 7) * (DM * 2) + (((s16 >> 1) ^ (rho & 7)) << 5) + (s16 & 1) * 16);
            }
#pragma unroll
        for (int j = 0; j < 8; ++j) { const int pc = lw + 4 * j, o = pc * 1024 + lane * 16, hd = o / 3840; glds16(xsrc, (unsigned)(hd * HEAD40 + (o - hd * 3840)), lds0 + K3_X + pc * 1024); }
#pragma unroll
        for (int t = 0; t < 3; ++t)
#pragma unroll
            for (int i = 0; i < 2; ++i) glds16(wsrc, wo[t][i], lds0 + K3_W + t * 8192 + (2 * lw + i) * 1024);
        k3_load_steps<0>(xsrc, wsrc, wo, lw, lane, lds0);
        asm volatile("s_barrier\n\ts_barrier" ::: "memory");
    } else {
        lds_cptr lds = (lds_cptr)smem;
        const int wm = wid >> 1, wn = wid & 1, m = lane & 15, g = lane >> 4, q = (lane & 15) >> 2, p = lane & 3;
        unsigned xlo[5], xhi[5], wa[4];
#pragma unroll
        for (int tt = 0; tt < 5; ++tt) {
            const int c = 4 * tt + g, P = c / 5, r = c - 5 * P, rb = (48 * wm + m) * 40;
            xlo[tt] = (unsigned)(K3_X + rb + (r < 4 ? (2 * P + (r >> 1)) * 3840 + (r & 1) * 16 : 2 * P * 3840 + 32));
            xhi[tt] = (unsigned)(K3_X + rb + (r < 4 ? (2 * P + (r >> 1)) * 3840 + (r & 1) * 16 + 8 : (2 * P + 1) * 3840 + 32));
        }
        { const int sx = (g & 1) * 4 + q;
#pragma unroll
          for (int j = 0; j < 4; ++j) wa[j] = (unsigned)(K3_W + ((g >> 1) * 16 + sx) * 256 + (((4 * wn + j) ^ sx) << 5) + p * 8); }
        f32x4 acc[3][4];
#pragma unroll
        for (int i = 0; i < 3; ++i)
#pragma unroll
            for (int j = 0; j < 4; ++j) acc[i][j] = f32x4{0.f, 0.f, 0.f, 0.f};
        bf16x8 xf[2][3], wf[2][4];
        auto load = [&](int t) {
            const int tt = t % 5, u = t / 5;
#pragma unroll
            for (int i = 0; i < 3; ++i) xf[t & 1][i] = cat8(lds_b64(lds + xlo[tt] + u * 30720 + i * 640), lds_b64(lds + xhi[tt] + u * 30720 + i * 640));
#pragma unroll
            for (int j = 0; j < 4; ++j) wf[t & 1][j] = cat8(lds_tr(lds + wa[j] + (t & 3) * 8192), lds_tr(lds + wa[j] + (t & 3) * 8192 + 2048));
        };
        STAMP(3, 0);
        BARRIER();
        STAMP(3, 1);
        load(0);
        SBAR();
#pragma unroll
        for (int t = 0; t < 20; ++t) {
            if (t + 1 < 20) { BARRIER(); load(t + 1); }
#pragma unroll
            for (int i = 0; i < 3; ++i)
#pragma unroll
                for (int j = 0; j < 4; ++j) acc[i][j] = MFMA16(wf[t & 1][j], xf[t & 1][i], acc[i][j]);
            if (t + 1 < 20) {
#pragma unroll
                for (int i = 0; i < 10; ++i) { __builtin_amdgcn_sched_group_barrier(0x008, 1, 0); __builtin_amdgcn_sched_group_barrier(0x100, 1, 0); }
                __builtin_amdgcn_sched_group_barrier(0x100, 4, 0); __builtin_amdgcn_sched_group_barrier(0x008, 2, 0);
            }
            SBAR();
        }
        STAMP(3, 2);
        BARRIER();
        STAMP(3, 3);
        lds_ptr st = (lds_ptr)smem;
#pragma unroll
        for (int i = 0; i < 3; ++i) {
            const int row = 48 * wm + 16 * i + m;
#pragma unroll
            for (int j = 0; j < 4; ++j) { const int b = (64 * wn + 16 * j + 4 * g) * 2; lds_st64(st + row * 256 + ((((b >> 4) ^ (row & 15)) << 4) | (b & 8)), pack4(acc[i][j])); }
        }
        BARRIER();
        STAMP(3, 4);
    }
#pragma unroll
    for (int it = 0; it < 3; ++it) {
        const int o = it * 8192 + tid * 16, row = o >> 8, s = (o >> 4) & 15;
        *(bf16x8*)(out + (size_t)(row0 + row) * 2560 + br * 1280 + ct * 256 + ((s ^ (row & 15)) << 4)) = lds_b128((lds_cptr)smem + o);
    }
    if (wid < 4) { STAMP(3, 5); DIAG_DRAIN(); STAMP(3, 7); }
}

constexpr int A_K = 0, A_V = 10240, A_SLOT = A_V + 11264, A_LDS = 84 * 1024;

#define MFMA32(a, b, c) __builtin_amdgcn_mfma_f32_32x32x16_bf16(a, b, c, 0, 0, 0)
#define MFMA32R(a, b, c) __builtin_amdgcn_mfma_f32_32x32x8bf16_1k(a, b, c, 0, 0, 0)

DI void attn_exact(const char* kf, const char* vf, bf16x8 qm, bf16x4 qr, int lane, f32x16& o) {
    const int r32 = lane & 31, h = lane >> 5;
    float mrun = -3.0e38f;
#pragma unroll
    for (int r = 0; r < 16; ++r) o[r] = 0.f;
#pragma unroll 1
    for (int t = 0; t < 64; ++t) {
        const bf16x8 km = *(const bf16x8*)(kf + t * 1280 + lane * 16);
        const bf16x4 kr = *(const bf16x4*)(kf + t * 1280 + 1024 + r32 * 8);
        const int vrow = r32 < 21 ? r32 : 20;
        const bf16x8 v0 = *(const bf16x8*)(vf + (2 * t) * 672 + h * 336 + vrow * 16);
        const bf16x8 v1 = *(const bf16x8*)(vf + (2 * t + 1) * 672 + h * 336 + vrow * 16);
        f32x16 s;
#pragma unroll
        for (int r = 0; r < 16; ++r) s[r] = 0.f;
        s = MFMA32(km, qm, s);
        s = MFMA32R(kr, qr, s);
        float mx = s[0];
#pragma unroll
        for (int r = 1; r < 16; ++r) mx = fmaxf(mx, s[r]);
        { auto rr = __builtin_amdgcn_permlane32_swap(__float_as_uint(mx), __float_as_uint(mx), false, false); mx = fmaxf(__uint_as_float(rr[0]), __uint_as_float(rr[1])); }
        const float mnew = fmaxf(mrun, mx), alpha = __builtin_amdgcn_exp2f(mrun - mnew);
        mrun = mnew;
        u32x4 p0, p1;
#pragma unroll
        for (int i = 0; i < 4; ++i) {
            p0[i] = cvtpk(__builtin_amdgcn_exp2f(s[2 * i] - mnew), __builtin_amdgcn_exp2f(s[2 * i + 1] - mnew));
            p1[i] = cvtpk(__builtin_amdgcn_exp2f(s[8 + 2 * i] - mnew), __builtin_amdgcn_exp2f(s[8 + 2 * i + 1] - mnew));
        }
#pragma unroll
        for (int r = 0; r < 16; ++r) o[r] *= alpha;
        o = MFMA32(v0, __builtin_bit_cast(bf16x8, p0), o);
        o = MFMA32(v1, __builtin_bit_cast(bf16x8, p1), o);
    }
}

DI float row_sum_of(const f32x16& o) {
    auto rr = __builtin_amdgcn_permlane32_swap(__float_as_uint(o[8]), __float_as_uint(o[8]), false, false);
    return __uint_as_float(rr[1]);
}

#define R_SA "v[64:79]"
#define R_SB "v[80:95]"
#define R_PL "v[100:103]"
#define R_PH "v[104:107]"
#define R_KM "v[108:111]"
#define R_KR "v[112:113]"
#define R_V0 "v[114:117]"
#define R_V1 "v[118:121]"
#define STR_(x) #x
#define STR(x) STR_(x)
#define MF16(d, a, b, c) "v_mfma_f32_32x32x16_bf16 " d ", " a ", " b ", " c "\n\t"
#define MF8(d, a, b, c) "v_mfma_f32_32x32x8_bf16 " d ", " a ", " b ", " c "\n\t"
#define CVT2(p0, p1) "v_cvt_pk_bf16_f32 v" STR(p0) ", v96, v97\n\tv_cvt_pk_bf16_f32 v" STR(p1) ", v98, v99\n\t"
#define EXP4(s0, s1, s2, s3) "v_exp_f32 v96, v" STR(s0) "\n\tv_exp_f32 v97, v" STR(s1) "\n\tv_exp_f32 v98, v" STR(s2) "\n\tv_exp_f32 v99, v" STR(s3) "\n\t"
#define GAP(p0, p1, s0, s1, s2, s3) CVT2(p0, p1) EXP4(s0, s1, s2, s3)
#define WAITL(n) "s_waitcnt lgkmcnt(" STR(n) ")\n\t"
#define RD128(d, a, off) "ds_read_b128 " d ", " a " offset:" STR(off) "\n\t"
#define RD64(d, a, off) "ds_read_b64 " d ", " a " offset:" STR(off) "\n\t"
#define RDV(d, off) "s_mov_b64 exec, s[48:49]\n\t" RD128(d, "%[vv]", off) "s_mov_b64 exec, -1\n\t"
#define RDKR(off) "s_mov_b64 exec, s[50:51]\n\t" RD64(R_KR, "%[vkr]", off) "s_mov_b64 exec, -1\n\t"
#define TILE(PRE_K, KOFF, V1OFF, PRE_V0, V0OFF) \
    MF16(R_SB, R_KM, "%[qbm]", "0") PRE_K RD128(R_KM, "%[vk]", KOFF) GAP(106, 107, 64, 65, 66, 67) \
    MF8(R_SB, R_KR, "%[qbr]", R_SB) RDKR(KOFF) GAP(100, 101, 68, 69, 70, 71) \
    MF16("%[ob]", R_V1, R_PH, "%[ob]") RDV(R_V1, V1OFF) GAP(102, 103, 72, 73, 74, 75) \
    WAITL(3) MF16("%[oa]", R_V0, R_PL, "%[oa]") GAP(104, 105, 76, 77, 78, 79) \
    WAITL(2) MF16(R_SA, R_KM, "%[qam]", "0") GAP(106, 107, 80, 81, 82, 83) \
    WAITL(1) MF8(R_SA, R_KR, "%[qar]", R_SA) GAP(100, 101, 84, 85, 86, 87) \
    WAITL(0) MF16("%[oa]", R_V1, R_PH, "%[oa]") GAP(102, 103, 88, 89, 90, 91) \
    MF16("%[ob]", R_V0, R_PL, "%[ob]") PRE_V0 RDV(R_V0, V0OFF) GAP(104, 105, 92, 93, 94, 95)

#define PRIO(n) "s_setprio " STR(n) "\n\t"
#define ASM_STAMP
#define DMA2(SLOTREG) \
    "v_add_u32 %[vd0], %[st0], %[vd0]\n\tv_add_u32 %[vd1], 0x1500, %[vd1]\n\t" \
    "s_add_u32 m0, %[d0], " SLOTREG "\n\ts_nop 0\n\tglobal_load_lds_dwordx4 %[vd0], %[ws]\n\t" \
    "s_cmp_ge_u32 %[wid], 5\n\ts_cbranch_scc1 5f\n\t" \
    "s_add_u32 m0, %[d0], " SLOTREG "\n\ts_add_u32 m0, m0, 0x4000\n\ts_nop 0\n\tglobal_load_lds_dwordx4 %[vd1], %[ws]\n\t" \
    "5:\n\t"

__global__ __launch_bounds__(1024, 4) void attn_kernel(char* ws) {
    extern __shared__ __attribute__((aligned(16))) char smem[];
    const int tid = threadIdx.x, lane = tid & 63, wid = (int)rfl(tid >> 6);
    const int xcd = blockIdx.x & 7, idx = blockIdx.x >> 3;
    const int bh = xcd + 8 * (idx >> 2), qblk = idx & 3;
    const int branch = (bh >> 3) & 1, head = (bh & 7) + 8 * (bh >> 4);
    const int kvh = branch ? 32 : head;
    const int qg = wid & 7, kh = wid >> 3;
    const int r32 = lane & 31, h = lane >> 5;
    const unsigned kf_off = KF_OFF + kvh * HEAD40, vf_off = VF_OFF + kvh * VF_HEAD;
    const unsigned lds0 = (unsigned)(uintptr_t)smem;
    STAMP(2, 0);
    auto v_src = [&](int p) { int o = (p - 10) * 1024 + lane * 16; o = o < 10752 ? o : 10736; return vf_off + (unsigned)(o < 5376 ? o : o - 5376 + 43008); };
    unsigned vd0 = wid < 10 ? kf_off + (unsigned)((wid >= 5 ? 40960 - 5120 : 0) + wid * 1024 + lane * 16) : v_src(wid);
    unsigned vd1 = v_src(wid + 16);
    const unsigned st0 = rfl(wid < 10 ? 5120u : 5376u), d0 = rfl(lds0 + wid * 1024);
    glds16(ws, vd0, d0);
    if (wid < 5) glds16(ws, vd1, d0 + 16384);
    const int qrow0 = qblk * 512 + qg * 64;
    const char* qa = ws + QF_OFF + (size_t)(branch * 32 + head) * HEAD40 + (size_t)(qrow0 + r32) * 40;
    const bf16x8 qam = *(const bf16x8*)(qa + 16 * h), qbm = *(const bf16x8*)(qa + 32 * 40 + 16 * h);
    bf16x4 qar = *(const bf16x4*)(qa + 32), qbr = *(const bf16x4*)(qa + 32 * 40 + 32);
    if (h) { qar = bf16x4{0, 0, 0, 0}; qbr = bf16x4{0, 0, 0, 0}; }
    f32x16 oa, ob;
#pragma unroll
    for (int r = 0; r < 16; ++r) { oa[r] = 0.f; ob[r] = 0.f; }
    STAMP(2, 1);
    unsigned vk = lds0 + A_K + kh * 5120 + lane * 16, vkr = lds0 + A_K + kh * 5120 + 1024 + r32 * 8, vv = lds0 + A_V + kh * 5376 + h * 336 + r32 * 16;
    asm volatile(
        "s_mov_b32 s44, 21504\n\t"
        "s_mov_b32 s48, 0x001fffff\n\ts_mov_b32 s49, 0x001fffff\n\ts_mov_b32 s50, -1\n\ts_mov_b32 s51, 0\n\t"
        "v_mov_b32 v96, 0\n\tv_mov_b32 v97, 0\n\tv_mov_b32 v98, 0\n\tv_mov_b32 v99, 0\n\t"
        "v_mov_b32 v104, 0\n\tv_mov_b32 v105, 0\n\tv_mov_b32 v112, 0\n\tv_mov_b32 v113, 0\n\t"
        "v_mov_b32 v114, 0\n\tv_mov_b32 v115, 0\n\tv_mov_b32 v116, 0\n\tv_mov_b32 v117, 0\n\t"
        "v_mov_b32 v118, 0\n\tv_mov_b32 v119, 0\n\tv_mov_b32 v120, 0\n\tv_mov_b32 v121, 0\n\t"
        "s_waitcnt vmcnt(0)\n\ts_barrier\n\t"
        DMA2("s44")
        RD128(R_KM, "%[vk]", 0) RDKR(0) RDV(R_V0, 0)
        "s_mov_b32 s40, 0\n\t"
        "s_mov_b32 s41, 0\n\t"
        WAITL(0)
        MF16(R_SA, R_KM, "%[qam]", "0") MF8(R_SA, R_KR, "%[qar]", R_SA)
        "s_nop 15\n\ts_nop 3\n\t"
        ASM_STAMP
        "2:\n\t"
        "s_add_u32 s42, s41, 21504\n\ts_cmp_eq_u32 s42, 64512\n\ts_cselect_b32 s42, 0, s42\n\t"
        "s_sub_u32 s43, s42, s41\n\t"
        "s_add_u32 s44, s42, 21504\n\ts_cmp_eq_u32 s44, 64512\n\ts_cselect_b32 s44, 0, s44\n\t"
        PRIO(3) TILE("", 1280, 672, "", 1344)
        PRIO(2) TILE("", 2560, 2016, "", 2688)
        PRIO(1) TILE("", 3840, 3360, "", 4032)
        PRIO(0) TILE(
            "s_waitcnt vmcnt(0)\n\ts_barrier\n\t"
            "v_add_u32 %[vk], s43, %[vk]\n\tv_add_u32 %[vkr], s43, %[vkr]\n\t"
            "s_cmp_ge_u32 s40, 6\n\ts_cbranch_scc1 3f\n\t"
            DMA2("s44")
            "3:\n\t",
            0, 4704, "v_add_u32 %[vv], s43, %[vv]\n\t", 0)
        "s_mov_b32 s41, s42\n\t"
        "s_add_u32 s40, s40, 1\n\t"
        "s_cmp_lt_u32 s40, 8\n\ts_cbranch_scc1 2b\n\t"
        "s_nop 1\n\t" CVT2(106, 107) "s_nop 1\n\t"
        MF16("%[ob]", R_V1, R_PH, "%[ob]")
        "s_waitcnt lgkmcnt(0)\n\ts_nop 15\n\ts_nop 3\n\t"
        : [oa] "+v"(oa), [ob] "+v"(ob), [vk] "+v"(vk), [vkr] "+v"(vkr), [vv] "+v"(vv), [vd0] "+v"(vd0), [vd1] "+v"(vd1)
        : [qam] "v"(qam), [qbm] "v"(qbm), [qar] "v"(qar), [qbr] "v"(qbr),
          [ws] "s"(ws), [d0] "s"(d0), [st0] "s"(st0), [wid] "s"(wid)
        : "memory", "scc", "s40", "s41", "s42", "s43", "s44", "s46", "s47", "s48", "s49", "s50", "s51",
          "v64", "v65", "v66", "v67", "v68", "v69", "v70", "v71", "v72", "v73", "v74", "v75", "v76", "v77", "v78", "v79",
          "v80", "v81", "v82", "v83", "v84", "v85", "v86", "v87", "v88", "v89", "v90", "v91", "v92", "v93", "v94", "v95",
          "v96", "v97", "v98", "v99", "v100", "v101", "v102", "v103", "v104", "v105", "v106", "v107", "v108", "v109", "v110", "v111",
          "v112", "v113", "v114", "v115", "v116", "v117", "v118", "v119", "v120", "v121");
    STAMP(2, 2);
    BARRIER();
    STAMP(2, 3);
    {
        lds_ptr xb = (lds_ptr)smem + (qg * 2 + kh) * 3072 + lane * 16;
        const f32x16 snd = kh ? oa : ob;
#pragma unroll
        for (int i = 0; i < 3; ++i) *(__attribute__((address_space(3))) f32x4*)(xb + i * 1024) = f32x4{snd[4 * i], snd[4 * i + 1], snd[4 * i + 2], snd[4 * i + 3]};
    }
    BARRIER();
    f32x16 o = kh ? ob : oa;
    {
        lds_cptr xb = (lds_cptr)smem + (qg * 2 + (kh ^ 1)) * 3072 + lane * 16;
#pragma unroll
        for (int i = 0; i < 3; ++i) { const f32x4 v = *(const __attribute__((address_space(3))) f32x4*)(xb + i * 1024);
#pragma unroll
            for (int e = 0; e < 4; ++e) o[4 * i + e] += v[e]; }
    }
    STAMP(2, 4);
    float l = row_sum_of(o);
    bool ok = l >= 0x1p-60f && l <= 0x1p60f;
#pragma unroll
    for (int r = 0; r < 8; ++r) ok = ok && (fabsf(o[r]) <= 3.0e38f);
    if (h == 0) {
#pragma unroll
        for (int r = 8; r < 12; ++r) ok = ok && (fabsf(o[r]) <= 3.0e38f);
    }
    if (__builtin_expect(!__all(ok), 0)) {
        attn_exact(ws + kf_off, ws + vf_off, kh ? qbm : qam, kh ? qbr : qar, lane, o);
        l = row_sum_of(o);
    }
    const float inv = 1.0f / l;
    char* orow = ws + OF_OFF + (size_t)(branch * 32 + head) * HEAD40 + (size_t)(qrow0 + 32 * kh + r32) * 40;
    *(u32x2*)(orow + 8 * h) = u32x2{cvtpk(o[0] * inv, o[1] * inv), cvtpk(o[2] * inv, o[3] * inv)};
    *(u32x2*)(orow + 16 + 8 * h) = u32x2{cvtpk(o[4] * inv, o[5] * inv), cvtpk(o[6] * inv, o[7] * inv)};
    if (h == 0) *(u32x2*)(orow + 32) = u32x2{cvtpk(o[8] * inv, o[9] * inv), cvtpk(o[10] * inv, o[11] * inv)};
    STAMP(2, 5);
    DIAG_DRAIN();
    STAMP(2, 7);
}

extern "C" void kernel_launch(void* const* d_in, const int* in_sizes, int n_in, void* d_out, int out_size, void* d_ws, size_t ws_size, hipStream_t stream) {
    (void)in_sizes; (void)n_in; (void)out_size; (void)ws_size;
    const char* xA = (const char*)d_in[0]; const char* wqkv = (const char*)d_in[1]; const char* woA = (const char*)d_in[2];
    const char* xB = (const char*)d_in[3]; const char* wq = (const char*)d_in[4]; const char* wk = (const char*)d_in[5]; const char* wv = (const char*)d_in[6]; const char* woB = (const char*)d_in[7];
    char* ws = (char*)d_ws;
    (void)hipFuncSetAttribute((const void*)proj_kernel, hipFuncAttributeMaxDynamicSharedMemorySize, K1_LDS);
    (void)hipFuncSetAttribute((const void*)attn_kernel, hipFuncAttributeMaxDynamicSharedMemorySize, A_LDS);
    (void)hipFuncSetAttribute((const void*)outproj_kernel, hipFuncAttributeMaxDynamicSharedMemorySize, K3_LDS);
    proj_kernel<<<256, 512, K1_LDS, stream>>>(xA, wqkv, xB, wq, wk, wv, ws);
    attn_kernel<<<256, 1024, A_LDS, stream>>>(ws);
    outproj_kernel<<<224, 512, K3_LDS, stream>>>(ws, woA, woB, (char*)d_out);
}
```

```cpp
#include <hip/hip_runtime.h>
#include <stdint.h>

typedef short bf16x8 __attribute__((ext_vector_type(8)));
typedef short bf16x4 __attribute__((ext_vector_type(4)));
typedef float f32x2 __attribute__((ext_vector_type(2)));
typedef float f32x4 __attribute__((ext_vector_type(4)));
typedef float f32x16 __attribute__((ext_vector_type(16)));
typedef unsigned u32x2 __attribute__((ext_vector_type(2)));
typedef unsigned u32x4 __attribute__((ext_vector_type(4)));
typedef __bf16 bf16x2_t __attribute__((ext_vector_type(2)));
typedef short v4i16_t __attribute__((ext_vector_type(4)));
typedef __attribute__((address_space(3))) const char* lds_cptr;
typedef __attribute__((address_space(3))) char* lds_ptr;

#define DI __device__ __forceinline__

constexpr int SEQ = 2048, DM = 640;
constexpr float QSCALE = 0.22360679774997896f * 1.4426950408889634f;

constexpr unsigned HEAD40 = SEQ * 40, KT = 1296, VB = 688, KF_HEAD = 64 * KT, VF_HEAD = 128 * VB;
constexpr unsigned QF_OFF = 0, KF_OFF = QF_OFF + 64 * HEAD40, VF_OFF = KF_OFF + 33 * KF_HEAD, OF_OFF = VF_OFF + 33 * VF_HEAD + 2048;

DI unsigned cvtpk(float lo, float hi) { f32x2 v = {lo, hi}; bf16x2_t b = __builtin_convertvector(v, bf16x2_t); return __builtin_bit_cast(unsigned, b); }
DI u32x2 pack4(f32x4 a) { return u32x2{cvtpk(a[0], a[1]), cvtpk(a[2], a[3])}; }
DI unsigned rfl(unsigned x) { return __builtin_amdgcn_readfirstlane(x); }
DI void glds16(const char* sbase, unsigned voff, unsigned lds_dst) {
    asm volatile("s_mov_b32 m0, %2\n\ts_nop 0\n\tglobal_load_lds_dwordx4 %0, %1" :: "v"(voff), "s"(sbase), "s"(lds_dst) : "memory");
}
template <int N> DI void wait_vm() { asm volatile("s_waitcnt vmcnt(%0)" :: "n"(N) : "memory"); }
DI bf16x8 lds_b128(lds_cptr p) { return *(const __attribute__((address_space(3))) bf16x8*)p; }
DI bf16x4 lds_b64(lds_cptr p) { return *(const volatile __attribute__((address_space(3))) bf16x4*)p; }
DI bf16x4 lds_tr(lds_cptr p) { return __builtin_bit_cast(bf16x4, __builtin_amdgcn_ds_read_tr16_b64_v4i16((__attribute__((address_space(3))) v4i16_t*)p)); }
DI void lds_st64(lds_ptr p, u32x2 v) { *(__attribute__((address_space(3))) u32x2*)p = v; }
DI bf16x8 cat8(bf16x4 a, bf16x4 b) { return __builtin_shufflevector(a, b, 0, 1, 2, 3, 4, 5, 6, 7); }
DI int swap23(int r) { return (r & ~12) | ((r & 4) << 1) | ((r & 8) >> 1); }
DI void touch_lines(const char* p, unsigned voff, unsigned lds_scratch) {
    asm volatile("s_mov_b32 m0, %2\n\ts_nop 0\n\tglobal_load_lds_dword %0, %1" :: "v"(voff), "s"(p), "s"(lds_scratch) : "memory");
}
DI void touch_addr(const char* p, unsigned lds_scratch) {
    asm volatile("s_mov_b32 m0, %1\n\ts_nop 0\n\tglobal_load_lds_dword %0, off" :: "v"(p), "s"(lds_scratch) : "memory");
}
#define CODE_ADDR(var, sym) asm volatile("s_getpc_b64 s[60:61]\n\ts_add_u32 s60, s60, " sym "@rel32@lo+4\n\ts_addc_u32 s61, s61, " sym "@rel32@hi+12\n\ts_mov_b64 %0, s[60:61]" : "=s"(var) :: "scc", "s60", "s61")
#define MFMA16(a, b, c) __builtin_amdgcn_mfma_f32_16x16x32_bf16(a, b, c, 0, 0, 0)
#define BARRIER() do { asm volatile("" ::: "memory"); __builtin_amdgcn_s_waitcnt(0xC07F); __builtin_amdgcn_s_barrier(); asm volatile("" ::: "memory"); } while (0)
#define SBAR() __builtin_amdgcn_sched_barrier(0)
#define K1_RPM 1
template <int NM, int NR> DI void interleave() {
#pragma unroll
    for (int i = 0; i < NR / K1_RPM; ++i) { __builtin_amdgcn_sched_group_barrier(0x008, 1, 0); __builtin_amdgcn_sched_group_barrier(0x100, K1_RPM, 0); }
    __builtin_amdgcn_sched_group_barrier(0x008, NM - NR / K1_RPM, 0);
    SBAR();
}
#define STAMP_DECL
#define STAMP(kern, slot)
#define STAMP_DUMP(kern)
#define DIAG_DRAIN()

constexpr int A_PART = 6144, A_SLOT = 4 * A_PART, A_X = 3 * A_SLOT, A_LDS = A_X + 16 * 3072;
static_assert(4 * KT <= A_PART && 8 * VB <= A_PART && A_SLOT == 24576, "stage layout");

#define MFMA32(a, b, c) __builtin_amdgcn_mfma_f32_32x32x16_bf16(a, b, c, 0, 0, 0)
#define MFMA32R(a, b, c) __builtin_amdgcn_mfma_f32_32x32x8bf16_1k(a, b, c, 0, 0, 0)

DI void attn_exact(const char* kf, const char* vf, bf16x8 qm, bf16x4 qr, int lane, f32x16& o) {
    const int r32 = lane & 31, h = lane >> 5;
    float mrun = -3.0e38f;
#pragma unroll
    for (int r = 0; r < 16; ++r) o[r] = 0.f;
#pragma unroll 1
    for (int t = 0; t < 64; ++t) {
        const bf16x8 km = *(const bf16x8*)(kf + t * KT + lane * 16);
        const bf16x4 kr = *(const bf16x4*)(kf + t * KT + 1024 + r32 * 8);
        const int vo = r32 < 21 ? h * 336 + r32 * 16 : 672;
        const bf16x8 v0 = *(const bf16x8*)(vf + (2 * t) * VB + vo);
        const bf16x8 v1 = *(const bf16x8*)(vf + (2 * t + 1) * VB + vo);
        f32x16 s;
#pragma unroll
        for (int r = 0; r < 16; ++r) s[r] = 0.f;
        s = MFMA32(km, qm, s);
        s = MFMA32R(kr, qr, s);
        float mx = s[0];
#pragma unroll
        for (int r = 1; r < 16; ++r) mx = fmaxf(mx, s[r]);
        { auto rr = __builtin_amdgcn_permlane32_swap(__float_as_uint(mx), __float_as_uint(mx), false, false); mx = fmaxf(__uint_as_float(rr[0]), __uint_as_float(rr[1])); }
        const float mnew = fmaxf(mrun, mx), alpha = __builtin_amdgcn_exp2f(mrun - mnew);
        mrun = mnew;
        u32x4 p0, p1;
#pragma unroll
        for (int i = 0; i < 4; ++i) {
            p0[i] = cvtpk(__builtin_amdgcn_exp2f(s[2 * i] - mnew), __builtin_amdgcn_exp2f(s[2 * i + 1] - mnew));
            p1[i] = cvtpk(__builtin_amdgcn_exp2f(s[8 + 2 * i] - mnew), __builtin_amdgcn_exp2f(s[8 + 2 * i + 1] - mnew));
        }
#pragma unroll
        for (int r = 0; r < 16; ++r) o[r] *= alpha;
        o = MFMA32(v0, __builtin_bit_cast(bf16x8, p0), o);
        o = MFMA32(v1, __builtin_bit_cast(bf16x8, p1), o);
    }
}

DI float row_sum_of(const f32x16& o) {
    auto rr = __builtin_amdgcn_permlane32_swap(__float_as_uint(o[8]), __float_as_uint(o[8]), false, false);
    return __uint_as_float(rr[1]);
}

#define R_SA "v[64:79]"
#define R_SB "v[80:95]"
#define R_PL "v[100:103]"
#define R_PH "v[104:107]"
#define R_KM "v[108:111]"
#define R_KR "v[112:113]"
#define R_V0 "v[114:117]"
#define R_V1 "v[118:121]"
#define STR_(x) #x
#define STR(x) STR_(x)
#define MF16(d, a, b, c) "v_mfma_f32_32x32x16_bf16 " d ", " a ", " b ", " c "\n\t"
#define MF8(d, a, b, c) "v_mfma_f32_32x32x8_bf16 " d ", " a ", " b ", " c "\n\t"
#define CVT2(p0, p1) "v_cvt_pk_bf16_f32 v" STR(p0) ", v96, v97\n\tv_cvt_pk_bf16_f32 v" STR(p1) ", v98, v99\n\t"
#define EXP4(s0, s1, s2, s3) "v_exp_f32 v96, v" STR(s0) "\n\tv_exp_f32 v97, v" STR(s1) "\n\tv_exp_f32 v98, v" STR(s2) "\n\tv_exp_f32 v99, v" STR(s3) "\n\t"
#define GAP(p0, p1, s0, s1, s2, s3) CVT2(p0, p1) EXP4(s0, s1, s2, s3)
#define WAITL(n) "s_waitcnt lgkmcnt(" STR(n) ")\n\t"
#define RD128(d, a, off) "ds_read_b128 " d ", " a " offset:" STR(off) "\n\t"
#define RD64(d, a, off) "ds_read_b64 " d ", " a " offset:" STR(off) "\n\t"
#define TILE(PRE_K, KOFF, V1OFF, PRE_V0, V0OFF) \
    MF16(R_SB, R_KM, "%[qbm]", "0") PRE_K RD128(R_KM, "v45", KOFF) GAP(106, 107, 64, 65, 66, 67) \
    MF8(R_SB, R_KR, "%[qbr]", R_SB) RD64(R_KR, "v46", KOFF) GAP(100, 101, 68, 69, 70, 71) \
    MF16("%[ob]", R_V1, R_PH, "%[ob]") RD128(R_V1, "v47", V1OFF) GAP(102, 103, 72, 73, 74, 75) \
    WAITL(3) MF16("%[oa]", R_V0, R_PL, "%[oa]") GAP(104, 105, 76, 77, 78, 79) \
    WAITL(2) MF16(R_SA, R_KM, "%[qam]", "0") GAP(106, 107, 80, 81, 82, 83) \
    WAITL(1) MF8(R_SA, R_KR, "%[qar]", R_SA) GAP(100, 101, 84, 85, 86, 87) \
    WAITL(0) MF16("%[oa]", R_V1, R_PH, "%[oa]") GAP(102, 103, 88, 89, 90, 91) \
    MF16("%[ob]", R_V0, R_PL, "%[ob]") RD128(R_V0, "v47", V0OFF) GAP(104, 105, 92, 93, 94, 95)
#define TILE_LAST(PRE_K, V1OFF, PRE_V0) \
    MF16(R_SB, R_KM, "%[qbm]", "0") PRE_K RD128(R_KM, "v45", 0) GAP(106, 107, 64, 65, 66, 67) \
    MF8(R_SB, R_KR, "%[qbr]", R_SB) RD64(R_KR, "v46", 0) GAP(100, 101, 68, 69, 70, 71) \
    MF16("%[ob]", R_V1, R_PH, "%[ob]") RD128(R_V1, "v47", V1OFF) GAP(102, 103, 72, 73, 74, 75) \
    WAITL(3) MF16("%[oa]", R_V0, R_PL, "%[oa]") GAP(104, 105, 76, 77, 78, 79) \
    WAITL(2) MF16(R_SA, R_KM, "%[qam]", "0") GAP(106, 107, 80, 81, 82, 83) \
    WAITL(1) MF8(R_SA, R_KR, "%[qar]", R_SA) GAP(100, 101, 84, 85, 86, 87) \
    WAITL(0) MF16("%[oa]", R_V1, R_PH, "%[oa]") GAP(102, 103, 88, 89, 90, 91) \
    MF16("%[ob]", R_V0, R_PL, "%[ob]") PRE_V0 RD128(R_V0, "v47", 0) GAP(104, 105, 92, 93, 94, 95)

#define PRIO(n) "s_setprio " STR(n) "\n\t"
#define ASM_STAMP(r)
#define DMA2(SLOTREG) \
    "s_lshr_b32 s46, s45, 1\n\ts_add_u32 s46, s46, %[blk]\n\ts_and_b32 s46, s46, 3\n\ts_lshl_b32 s46, s46, 2\n\t" \
    "s_and_b32 s47, s45, 1\n\ts_add_u32 s46, s46, s47\n\ts_lshl_b32 s46, s46, 2\n\ts_add_u32 s45, s45, 1\n\t" \
    "s_mul_i32 s47, s46, %[p0]\n\ts_add_u32 s60, s48, s47\n\ts_addc_u32 s61, s49, 0\n\t" \
    "s_add_u32 m0, %[d0], " SLOTREG "\n\ts_cmp_ge_u32 %[wid], 8\n\tglobal_load_lds_dwordx4 v40, s[60:61]\n\t" \
    "s_cbranch_scc1 5f\n\t" \
    "s_mul_i32 s47, s46, 1376\n\ts_add_u32 s60, s50, s47\n\ts_addc_u32 s61, s51, 0\n\t" \
    "s_add_u32 m0, %[d0], " SLOTREG "\n\ts_add_u32 m0, m0, 0x4000\n\ts_nop 0\n\tglobal_load_lds_dwordx4 v40, s[60:61]\n\t" \
    "5:\n\t"

extern "C" __global__ __launch_bounds__(1024, 4) void attn_kernel(char* ws, const char* woA, const char* woB) {
    extern __shared__ __attribute__((aligned(16))) char smem[];
    const int tid = threadIdx.x, lane = tid & 63, wid = (int)rfl(tid >> 6);
    const int xcd = blockIdx.x & 7, idx = blockIdx.x >> 3;
    const int qblk = xcd & 3, branch = idx >> 4, head = 16 * (xcd >> 2) + (idx & 15);
    const int kvh = branch ? 32 : head;
    const int qg = wid & 7, kh = wid >> 3;
    const unsigned kf_off = KF_OFF + kvh * KF_HEAD, vf_off = VF_OFF + kvh * VF_HEAD;
    const unsigned lds0 = (unsigned)(uintptr_t)smem;
    STAMP_DECL;
    STAMP(2, 0);
    const int pa = wid / 6, pb = (wid + 16) / 6;
    const unsigned src0 = rfl((pa < 2 ? kf_off + pa * 8 * KT : vf_off) + (wid - 6 * pa) * 1024), pitch0 = rfl(pa < 2 ? KT : 2 * VB);
    const unsigned src1 = rfl(vf_off + (pb - 2) * 16 * VB + (wid + 16 - 6 * pb) * 1024);
    const unsigned d0 = rfl(lds0 + wid * 1024);
    const int qrow0 = qblk * 512 + qg * 64;
    const unsigned qoff = rfl(QF_OFF + (unsigned)(branch * 32 + head) * HEAD40 + (unsigned)qrow0 * 40);
    const unsigned kbase = rfl(lds0 + kh * A_PART), vbase = rfl(lds0 + 2 * A_PART + kh * A_PART);
    bf16x8 qam, qbm; bf16x4 qar, qbr;
    f32x16 oa, ob;
    asm volatile(
        ASM_STAMP("s[72:73]")
        "v_lshlrev_b32 v40, 4, %[lane]\n\t"
        "s_mov_b64 s[52:53], %[ws]\n\t"
        "s_add_u32 s48, s52, %[src0]\n\ts_addc_u32 s49, s53, 0\n\ts_add_u32 s50, s52, %[src1]\n\ts_addc_u32 s51, s53, 0\n\t"
        "s_mov_b32 s44, 0\n\ts_mov_b32 s45, 0\n\t"
        DMA2("s44") ASM_STAMP("s[64:65]")
        "v_and_b32 v41, 31, %[lane]\n\tv_lshrrev_b32 v42, 5, %[lane]\n\t"
        "s_add_u32 s62, s52, %[qoff]\n\ts_addc_u32 s63, s53, 0\n\t"
        "v_lshlrev_b32 v43, 3, v41\n\t"
        "v_mov_b64 %[qar], 0\n\tv_mov_b64 %[qbr], 0\n\t"
        "global_load_dwordx4 %[qam], v40, s[62:63]\n\tglobal_load_dwordx4 %[qbm], v40, s[62:63] offset:1280\n\t"
        "s_mov_b32 exec_hi, 0\n\t"
        "global_load_dwordx2 %[qar], v43, s[62:63] offset:1024\n\tglobal_load_dwordx2 %[qbr], v43, s[62:63] offset:2304\n\t"
        "s_mov_b32 exec_hi, -1\n\t"
        "v_cmp_eq_u32 vcc, 0, v42\n\t"
        "v_add_u32 v45, %[kbase], v40\n\t"
        "v_add_u32 v46, 0x400, v43\n\tv_mov_b32 v48, 0x500\n\ts_nop 0\n\t"
        "v_cndmask_b32 v46, v48, v46, vcc\n\tv_add_u32 v46, %[kbase], v46\n\t"
        "v_cmp_gt_u32 vcc, 21, v41\n\t"
        "v_mul_u32_u24 v47, 0x150, v42\n\tv_lshl_add_u32 v47, v41, 4, v47\n\tv_mov_b32 v48, 0x2a0\n\t"
        "v_cndmask_b32 v47, v48, v47, vcc\n\tv_add_u32 v47, %[vbase], v47\n\t"
        "v_mov_b32 v118, 0\n\tv_mov_b32 v119, 0\n\tv_mov_b32 v120, 0\n\tv_mov_b32 v121, 0\n\t"
        "v_mov_b32 v96, 0\n\tv_mov_b32 v97, 0\n\tv_mov_b32 v98, 0\n\tv_mov_b32 v99, 0\n\t"
        "v_mov_b32 v104, 0\n\tv_mov_b32 v105, 0\n\t"
        "v_mfma_f32_32x32x16_bf16 %[oa], v[118:121], v[118:121], 0\n\tv_mfma_f32_32x32x16_bf16 %[ob], v[118:121], v[118:121], 0\n\t"
        "s_mov_b32 s40, 0\n\t"
        "s_mov_b32 s41, 0\n\t"
        "s_mov_b32 s44, 24576\n\t"
        "s_waitcnt vmcnt(4)\n\t" ASM_STAMP("s[66:67]") "s_barrier\n\t" ASM_STAMP("s[68:69]")
        RD128(R_KM, "v45", 0) RD64(R_KR, "v46", 0) RD128(R_V0, "v47", 0)
        "s_waitcnt vmcnt(0)\n\t"
        DMA2("s44")
        WAITL(0)
        MF16(R_SA, R_KM, "%[qam]", "0") MF8(R_SA, R_KR, "%[qar]", R_SA)
        "s_nop 15\n\ts_nop 3\n\t"
        ASM_STAMP("s[70:71]")
        "2:\n\t"
        "s_add_u32 s42, s41, 24576\n\ts_cmp_eq_u32 s42, 73728\n\ts_cselect_b32 s42, 0, s42\n\t"
        "s_sub_u32 s43, s42, s41\n\t"
        "s_add_u32 s44, s42, 24576\n\ts_cmp_eq_u32 s44, 73728\n\ts_cselect_b32 s44, 0, s44\n\t"
        PRIO(3) TILE("", 1296, 688, "", 1376)
        PRIO(2) TILE("", 2592, 2064, "", 2752)
        PRIO(1) TILE("", 3888, 3440, "", 4128)
        PRIO(0) TILE_LAST(
            "s_waitcnt vmcnt(0)\n\ts_barrier\n\t"
            "v_add_u32 v45, s43, v45\n\tv_add_u32 v46, s43, v46\n\t"
            "s_cmp_ge_u32 s40, 6\n\ts_cbranch_scc1 3f\n\t"
            DMA2("s44")
            "3:\n\t",
            4816, "v_add_u32 v47, s43, v47\n\t")
        "s_mov_b32 s41, s42\n\t"
        "s_add_u32 s40, s40, 1\n\t"
        "s_cmp_lt_u32 s40, 8\n\ts_cbranch_scc1 2b\n\t"
        "s_nop 1\n\t" CVT2(106, 107) "s_nop 1\n\t"
        MF16("%[ob]", R_V1, R_PH, "%[ob]")
        "s_waitcnt lgkmcnt(0)\n\ts_nop 15\n\ts_nop 3\n\t"
        : [oa] "=&v"(oa), [ob] "=&v"(ob), [qam] "=&v"(qam), [qbm] "=&v"(qbm), [qar] "=&v"(qar), [qbr] "=&v"(qbr)
        : [lane] "v"(lane), [ws] "s"(ws), [src0] "s"(src0), [src1] "s"(src1), [p0] "s"(pitch0), [blk] "s"(qblk), [qoff] "s"(qoff),
          [kbase] "s"(kbase), [vbase] "s"(vbase), [d0] "s"(d0), [wid] "s"(wid)
        : "memory", "scc", "vcc", "s40", "s41", "s42", "s43", "s44", "s45", "s46", "s47", "s48", "s49", "s50", "s51", "s52", "s53", "s60", "s61", "s62", "s63",
          "s64", "s65", "s66", "s67", "s68", "s69", "s70", "s71", "s72", "s73",
          "v40", "v41", "v42", "v43", "v44", "v45", "v46", "v47", "v48",
          "v64", "v65", "v66", "v67", "v68", "v69", "v70", "v71", "v72", "v73", "v74", "v75", "v76", "v77", "v78", "v79",
          "v80", "v81", "v82", "v83", "v84", "v85", "v86", "v87", "v88", "v89", "v90", "v91", "v92", "v93", "v94", "v95",
          "v96", "v97", "v98", "v99", "v100", "v101", "v102", "v103", "v104", "v105", "v106", "v107", "v108", "v109", "v110", "v111",
          "v112", "v113", "v114", "v115", "v116", "v117", "v118", "v119", "v120", "v121");
    STAMP(2, 2);
    const int r32 = lane & 31, h = lane >> 5;
    if (wid < 4) { const int l0 = idx * 200 + wid * 50; touch_lines((xcd >> 2 ? woB : woA) + l0 * 128, (lane < 50 ? lane : 49) * 128, rfl(lds0 + wid * 256)); }
    else if (wid < 6) { const char* code; CODE_ADDR(code, "outproj_kernel"); touch_lines(code + (wid - 4) * 8192, lane * 128, rfl(lds0 + wid * 256)); }
    touch_lines(ws + OF_OFF + (size_t)((unsigned)(branch * 32 + head) * HEAD40 + (unsigned)(qrow0 + 32 * kh) * 40), (lane < 10 ? lane : 9) * 128, rfl(lds0 + 2048 + wid * 256));
    f32x16 o;
    {
        lds_ptr xb = (lds_ptr)smem + A_X + wid * 3072 + lane * 16;
        if (kh) {
#pragma unroll
            for (int i = 0; i < 3; ++i) *(__attribute__((address_space(3))) f32x4*)(xb + i * 1024) = f32x4{oa[4 * i], oa[4 * i + 1], oa[4 * i + 2], oa[4 * i + 3]};
            o = ob;
        } else {
#pragma unroll
            for (int i = 0; i < 3; ++i) *(__attribute__((address_space(3))) f32x4*)(xb + i * 1024) = f32x4{ob[4 * i], ob[4 * i + 1], ob[4 * i + 2], ob[4 * i + 3]};
            o = oa;
        }
    }
    BARRIER();
    {
        lds_cptr xb = (lds_cptr)smem + A_X + (wid ^ 8) * 3072 + lane * 16;
#pragma unroll
        for (int i = 0; i < 3; ++i) { const f32x4 v = *(const __attribute__((address_space(3))) f32x4*)(xb + i * 1024);
#pragma unroll
            for (int e = 0; e < 4; ++e) o[4 * i + e] += v[e]; }
    }
    float l = row_sum_of(o);
    float mag = 0.f;
#pragma unroll
    for (int r = 0; r < 12; ++r) mag += fabsf(o[r]);
    const bool ok = l >= 0x1p-60f && l <= 0x1p60f && mag <= 3.0e38f;
    if (__builtin_expect(!__all(ok), 0)) {
        attn_exact(ws + kf_off, ws + vf_off, kh ? qbm : qam, kh ? qbr : qar, lane, o);
        l = row_sum_of(o);
    }
    const float inv = __builtin_amdgcn_rcpf(l);
    char* orow = ws + OF_OFF + (size_t)((unsigned)(branch * 32 + head) * HEAD40 + (unsigned)(qrow0 + 32 * kh + r32) * 40);
    *(u32x2*)(orow + 8 * h) = u32x2{cvtpk(o[0] * inv, o[1] * inv), cvtpk(o[2] * inv, o[3] * inv)};
    *(u32x2*)(orow + 16 + 8 * h) = u32x2{cvtpk(o[4] * inv, o[5] * inv), cvtpk(o[6] * inv, o[7] * inv)};
    if (h == 0) *(u32x2*)(orow + 32) = u32x2{cvtpk(o[8] * inv, o[9] * inv), cvtpk(o[10] * inv, o[11] * inv)};
    DIAG_DRAIN();
    STAMP(2, 7);
    STAMP_DUMP(2);
}

#define K3_RPM 2
constexpr int K3_X = 0, K3_W = 32 * 3840, K3_LDS = K3_W + 4 * 8192;
constexpr int k3_total(int t) { int n = 14; for (int u = 0; u < t; ++u) n += (u <= 16 ? 2 : 0) + (u <= 10 ? 2 : 0); return n; }
constexpr int k3_allowed(int t) {
    int last = t <= 2 ? 10 + 2 * t : k3_total(t - 3) + 2;
    const int xg = t == 5 ? 3 : t == 10 ? 7 : t == 15 ? 10 : -1;
    if (xg >= 0) { const int e = k3_total(xg) + 4; if (e > last) last = e; }
    return k3_total(t) - last;
}
DI int k3_krow(int c, int e) { const int P = c / 5, r = c - 5 * P; return r < 4 ? (2 * P + (r >> 1)) * 20 + (r & 1) * 8 + e : (2 * P + (e >> 2)) * 20 + 16 + (e & 3); }

DI unsigned k3_xoff(int o, int rot) { const int hd = o / 3840; return (unsigned)(((hd + 8 * rot) & 31) * HEAD40 + (o - hd * 3840)); }
template <int T> DI void k3_load_steps(const char* xsrc, const char* wsrc, const unsigned (&wo)[5][2], int lw, int lane, unsigned lds0, int rot) {
    if constexpr (T < 20) {
        wait_vm<k3_allowed(T)>();
        asm volatile("s_barrier" ::: "memory");
        if constexpr (T + 3 < 20) {
            constexpr int t = T + 3;
#pragma unroll
            for (int i = 0; i < 2; ++i) glds16(wsrc + (size_t)((t / 5 + rot) & 3) * 160 * (DM * 2), wo[t % 5][i], lds0 + K3_W + (t & 3) * 8192 + (2 * lw + i) * 1024);
        }
        if constexpr (T <= 10) {
#pragma unroll
            for (int i = 0; i < 2; ++i) { const int pc = lw + 4 * (8 + 2 * T + i); glds16(xsrc, k3_xoff(pc * 1024 + lane * 16, rot), lds0 + K3_X + pc * 1024); }
        }
        k3_load_steps<T + 1>(xsrc, wsrc, wo, lw, lane, lds0, rot);
    }
}

extern "C" __global__ __launch_bounds__(512, 2) void outproj_kernel(char* ws, const char* woA, const char* woB, char* out) {
    extern __shared__ __attribute__((aligned(16))) char smem[];
    const int tid = threadIdx.x, lane = tid & 63, wid = (int)rfl(tid >> 6);
    const int xcd = blockIdx.x & 7, idx = blockIdx.x >> 3, blk = xcd & 3, br = xcd >> 2;
    if (idx >= 30) return;
    STAMP_DECL;
    const int rl = idx / 5, ct = idx - 5 * rl, row0 = blk * 512 + (rl < 5 ? rl * 96 : 416), rot = 2 * br;
    if (wid >= 4) {
        const int lw = wid - 4;
        const char* xsrc = ws + OF_OFF + (size_t)br * 32 * HEAD40 + row0 * 40;
        const char* wsrc = (br ? woB : woA) + ct * 256;
        const unsigned lds0 = (unsigned)(uintptr_t)smem;
        unsigned wo[5][2];
#pragma unroll
        for (int tt = 0; tt < 5; ++tt)
#pragma unroll
            for (int i = 0; i < 2; ++i) {
                const int rho = 4 * (2 * lw + i) + (lane >> 4), s16 = lane & 15, kap = swap23(rho);
                wo[tt][i] = (unsigned)(k3_krow(4 * tt + (kap >> 3), kap & 7) * (DM * 2) + (((s16 >> 1) ^ (rho & 7)) << 5) + (s16 & 1) * 16);
            }
#pragma unroll
        for (int j = 0; j < 8; ++j) { const int pc = lw + 4 * j; glds16(xsrc, k3_xoff(pc * 1024 + lane * 16, rot), lds0 + K3_X + pc * 1024); }
#pragma unroll
        for (int t = 0; t < 3; ++t)
#pragma unroll
            for (int i = 0; i < 2; ++i) glds16(wsrc + (size_t)rot * 160 * (DM * 2), wo[t][i], lds0 + K3_W + t * 8192 + (2 * lw + i) * 1024);
        k3_load_steps<0>(xsrc, wsrc, wo, lw, lane, lds0, rot);
        asm volatile("s_barrier\n\ts_barrier" ::: "memory");
    } else {
        lds_cptr lds = (lds_cptr)smem;
        const int wm = wid >> 1, wn = wid & 1, m = lane & 15, g = lane >> 4, q = (lane & 15) >> 2, p = lane & 3;
        unsigned xlo[5], xhi[5], wa[4];
#pragma unroll
        for (int tt = 0; tt < 5; ++tt) {
            const int c = 4 * tt + g, P = c / 5, r = c - 5 * P, rb = (48 * wm + m) * 40;
            xlo[tt] = (unsigned)(K3_X + rb + (r < 4 ? (2 * P + (r >> 1)) * 3840 + (r & 1) * 16 : 2 * P * 3840 + 32));
            xhi[tt] = (unsigned)(K3_X + rb + (r < 4 ? (2 * P + (r >> 1)) * 3840 + (r & 1) * 16 + 8 : (2 * P + 1) * 3840 + 32));
        }
        { const int sx = (g & 1) * 4 + q;
#pragma unroll
          for (int j = 0; j < 4; ++j) wa[j] = (unsigned)(K3_W + ((g >> 1) * 16 + sx) * 256 + (((4 * wn + j) ^ sx) << 5) + p * 8); }
        f32x4 acc[3][4];
#pragma unroll
        for (int i = 0; i < 3; ++i)
#pragma unroll
            for (int j = 0; j < 4; ++j) acc[i][j] = f32x4{0.f, 0.f, 0.f, 0.f};
        bf16x8 xf[2][3], wf[2][4];
        auto load = [&](int t) {
            const int tt = t % 5, u = t / 5;
#pragma unroll
            for (int i = 0; i < 3; ++i) xf[t & 1][i] = cat8(lds_b64(lds + xlo[tt] + u * 30720 + i * 640), lds_b64(lds + xhi[tt] + u * 30720 + i * 640));
#pragma unroll
            for (int j = 0; j < 4; ++j) wf[t & 1][j] = cat8(lds_tr(lds + wa[j] + (t & 3) * 8192), lds_tr(lds + wa[j] + (t & 3) * 8192 + 2048));
        };
        STAMP(3, 0);
        BARRIER();
        STAMP(3, 1);
        load(0);
        SBAR();
#pragma unroll
        for (int t = 0; t < 20; ++t) {
            if (t + 1 < 20) { BARRIER(); load(t + 1); }
#pragma unroll
            for (int i = 0; i < 3; ++i)
#pragma unroll
                for (int j = 0; j < 4; ++j) acc[i][j] = MFMA16(wf[t & 1][j], xf[t & 1][i], acc[i][j]);
            if (t + 1 < 20) {
#pragma unroll
                for (int i = 0; i < 14 / K3_RPM; ++i) { __builtin_amdgcn_sched_group_barrier(0x100, K3_RPM, 0); __builtin_amdgcn_sched_group_barrier(0x008, 1, 0); }
                __builtin_amdgcn_sched_group_barrier(0x100, 14, 0); __builtin_amdgcn_sched_group_barrier(0x008, 12, 0);
            }
            SBAR();
        }
        STAMP(3, 2);
        BARRIER();
        STAMP(3, 3);
        lds_ptr st = (lds_ptr)smem;
#pragma unroll
        for (int i = 0; i < 3; ++i) {
            const int row = 48 * wm + 16 * i + m;
#pragma unroll
            for (int j = 0; j < 4; ++j) { const int b = (64 * wn + 16 * j + 4 * g) * 2; lds_st64(st + row * 256 + ((((b >> 4) ^ (row & 15)) << 4) | (b & 8)), pack4(acc[i][j])); }
        }
        BARRIER();
        STAMP(3, 4);
    }
#pragma unroll
    for (int it = 0; it < 3; ++it) {
        const int o = it * 8192 + tid * 16, row = o >> 8, s = (o >> 4) & 15;
        *(bf16x8*)(out + (size_t)(row0 + row) * 2560 + br * 1280 + ct * 256 + ((s ^ (row & 15)) << 4)) = lds_b128((lds_cptr)smem + o);
    }
    if (wid < 4) { STAMP(3, 5); DIAG_DRAIN(); STAMP(3, 7); STAMP_DUMP(3); }
}

constexpr int NSTAGE = DM / 64;
constexpr int K1_X = 0, K1_W = 16384, K1_W2 = K1_W + 24576, K1_SLOT = K1_W2 + 3072, K1_LDS = 3 * K1_SLOT + 256;

template <int NP> DI void k1_loader(const char* xg, const char* wg, const char* w2g, int wpitch, int lw, int lane, unsigned lds0, const char* tdst, unsigned dstride) {
    unsigned xo[4], wo[6], w2o = 0;
#pragma unroll
    for (int i = 0; i < 4; ++i) { const int row = (4 * lw + i) * 8 + (lane >> 3), pos = lane & 7; xo[i] = (unsigned)(row * (DM * 2) + ((pos ^ ((row >> 1) & 7)) << 4)); }
#pragma unroll
    for (int i = 0; i < 6; ++i) {
        const int o = (6 * lw + i) * 1024 + lane * 16, rho = o / 384, b = o - rho * 384;
        wo[i] = (unsigned)(swap23(rho) * wpitch + (((b >> 5) ^ ((rho >> 1) & 3)) << 5) + (b & 16));
    }
    const int p2 = lw < 3 ? lw : 2;
    if (NP == 11) { const int o = p2 * 1024 + lane * 16; w2o = (unsigned)(o < 2560 ? o : 2544); }
    auto issue = [&](int s, int slot) {
        const unsigned sl = lds0 + (unsigned)slot * K1_SLOT;
        const char* xs = xg + (size_t)s * 128; const char* wsrc = wg + (size_t)s * 64 * wpitch;
#pragma unroll
        for (int i = 0; i < 4; ++i) glds16(xs, xo[i], sl + K1_X + (4 * lw + i) * 1024);
#pragma unroll
        for (int i = 0; i < 6; ++i) glds16(wsrc, wo[i], sl + K1_W + (6 * lw + i) * 1024);
        if (NP == 11) glds16(w2g + (size_t)s * 2560, w2o, sl + K1_W2 + p2 * 1024);
    };
    issue(0, 0); issue(1, 1);
    int nslot = 2;
#pragma unroll 1
    for (int s = 0; s < NSTAGE; ++s) {
        if (s < NSTAGE - 1) wait_vm<NP>(); else wait_vm<0>();
        asm volatile("s_barrier" ::: "memory");
        if (s + 2 < NSTAGE) { issue(s + 2, nslot); nslot = nslot == 2 ? 0 : nslot + 1; }
    }
}

#define TOUCH_ALL 0
struct K1Frag { bf16x8 x[4]; bf16x8 w[5]; bf16x8 w2; };

#define STP_PARAM
#define STP_ARG
template <int KIND, int XTRA> DI void k1_consumer(char* ws, char* smem, int wid, int lane, int shift32, int xo, int row0, const char* dst, unsigned dstride STP_PARAM) {
    lds_cptr lds = (lds_cptr)smem;
    const int wm = wid >> 1, wn = wid & 1, m = lane & 15, g = lane >> 4, q = (lane & 15) >> 2, p = lane & 3;
    const unsigned xa0 = K1_X + (unsigned)((64 * wm + m) * 128 + ((g ^ ((m >> 1) & 7)) << 4));
    unsigned wa[5];
    { const int gx = ((g & 1) << 1) | (q >> 1);
#pragma unroll
      for (int j = 0; j < 5; ++j) wa[j] = K1_W + (unsigned)(((g >> 1) * 16 + (g & 1) * 4 + q) * 384 + (((shift32 + 5 * wn + j) ^ gx) << 5) + p * 8); }
    const unsigned w2a = K1_W2 + (unsigned)((8 * g + q) * 40 + p * 8 + xo * 2);
    f32x4 acc[4][5], acc2[2];
#pragma unroll
    for (int i = 0; i < 4; ++i)
#pragma unroll
        for (int j = 0; j < 5; ++j) acc[i][j] = f32x4{0.f, 0.f, 0.f, 0.f};
    acc2[0] = acc2[1] = f32x4{0.f, 0.f, 0.f, 0.f};
    auto load = [&](K1Frag& f, unsigned sl, int kk) {
        lds_cptr b = lds + sl;
#pragma unroll
        for (int i = 0; i < 4; ++i) f.x[i] = lds_b128(b + ((xa0 ^ (kk * 64)) + i * 2048));
#pragma unroll
        for (int j = 0; j < 5; ++j) f.w[j] = cat8(lds_tr(b + wa[j] + kk * 12288), lds_tr(b + wa[j] + kk * 12288 + 3072));
        if (XTRA) f.w2 = cat8(lds_tr(b + w2a + kk * 1280), lds_tr(b + w2a + kk * 1280 + 160));
    };
    auto mma = [&](const K1Frag& f) {
#pragma unroll
        for (int i = 0; i < 4; ++i)
#pragma unroll
            for (int j = 0; j < 5; ++j) acc[i][j] = KIND == 2 ? MFMA16(f.x[i], f.w[j], acc[i][j]) : MFMA16(f.w[j], f.x[i], acc[i][j]);
        if (XTRA) {
#pragma unroll
            for (int i = 0; i < 2; ++i) { const bf16x8 xx = wn ? f.x[2 + i] : f.x[i]; acc2[i] = XTRA == 2 ? MFMA16(xx, f.w2, acc2[i]) : MFMA16(f.w2, xx, acc2[i]); }
        }
    };
    constexpr int CH = KIND == 0 ? 5120 : KIND == 1 ? 4 * KT : 8 * VB, TP = KIND == 0 ? 1280 : KT;
    unsigned sa[5], sh[5];
#pragma unroll
    for (int j = 0; j < 5; ++j) {
        const int col = 80 * wn + 16 * j + (KIND == 2 ? m : 4 * g), hl = (col * 205) >> 12, d = col - hl * 20;
        if (KIND == 2) { sa[j] = (unsigned)(hl * CH + 4 * wm * VB + (g & 1) * 336 + (g >> 1) * 8 + d * 16); sh[j] = 0; }
        else { const bool rem = d == 16; sa[j] = (unsigned)(hl * CH + 2 * wm * TP + (rem ? 1024 + m * 8 : (d >> 3) * 512 + m * 16 + (d & 4) * 2)); sh[j] = sa[j] + (rem ? 128 : 256); asm volatile("" : "+v"(sh[j])); }
        asm volatile("" : "+v"(sa[j]));
    }
    K1Frag f0, f1;
    STAMP(1, 0);
    BARRIER();
    STAMP(1, 1);
    const unsigned scratch = rfl((unsigned)(uintptr_t)smem + 3 * K1_SLOT);
    load(f0, 0, 0);
    int slot = 0;
    constexpr int NM = XTRA ? 22 : 20, NR = XTRA ? 16 : 14;
    SBAR();
#pragma unroll 1
    for (int s = 0; s < NSTAGE - 1; ++s) {
        load(f1, (unsigned)slot * K1_SLOT, 1);
        mma(f0);
        interleave<NM, NR>();
        slot = slot == 2 ? 0 : slot + 1;
        BARRIER();
        if (wid == (s & 3)) {
            if (s == 8) { const char* code; CODE_ADDR(code, "attn_kernel"); touch_lines(code, lane * 128, scratch); }
            SBAR();
        }
        load(f0, (unsigned)slot * K1_SLOT, 0);
        mma(f1);
        interleave<NM, NR>();
    }
    load(f1, (unsigned)slot * K1_SLOT, 1);
    mma(f0);
    interleave<NM, NR>();
    mma(f1);
    STAMP(1, 2);
    BARRIER();
    STAMP(1, 3);
    lds_ptr st = (lds_ptr)smem;
#pragma unroll
    for (int j = 0; j < 5; ++j) {
        if (KIND == 2) {
#pragma unroll
            for (int i = 0; i < 4; ++i) lds_st64(st + sa[j] + i * VB, pack4(acc[i][j]));
        } else {
            const float sc = KIND == 0 ? QSCALE : 1.0f;
            lds_st64(st + sa[j], pack4(acc[0][j] * sc)); lds_st64(st + sa[j] + TP, pack4(acc[2][j] * sc));
            lds_st64(st + sh[j], pack4(acc[1][j] * sc)); lds_st64(st + sh[j] + TP, pack4(acc[3][j] * sc));
        }
    }
    if (XTRA == 1) {
        const int d0 = xo + 4 * g;
        if (d0 < (xo ? 20 : 12)) {
#pragma unroll
            for (int i = 0; i < 2; ++i) {
                const int row = row0 + 64 * wm + 32 * wn + 16 * i + m, key = row & 31;
                *(u32x2*)(ws + KF_OFF + (size_t)32 * KF_HEAD + (row >> 5) * KT + (d0 < 16 ? (d0 >> 3) * 512 + key * 16 + (d0 & 4) * 2 : 1024 + key * 8)) = pack4(acc2[i]);
            }
        }
        if (xo == 0 && lane == 0 && wn == 0) {
#pragma unroll
            for (int i = 0; i < 2; ++i) *(u32x4*)(ws + KF_OFF + (size_t)32 * KF_HEAD + ((row0 >> 5) + 2 * wm + i) * KT + 1280) = u32x4{0u, 0u, 0u, 0u};
        }
    }
    if (XTRA == 2) {
        const int d = xo + m;
#pragma unroll
        for (int i = 0; i < 2; ++i) {
            char* dst = ws + VF_OFF + (size_t)32 * VF_HEAD + ((row0 >> 4) + 4 * wm + 2 * wn + i) * VB;
            if (d < (xo ? 20 : 12)) *(u32x2*)(dst + (g & 1) * 336 + (g >> 1) * 8 + d * 16) = pack4(acc2[i]);
            if (xo == 0 && m == 15) *(u32x2*)(dst + (g & 1) * 336 + (g >> 1) * 8 + 320) = u32x2{0x3F803F80u, 0x3F803F80u};
            if (xo == 0 && lane == 14) *(u32x4*)(dst + 672) = u32x4{0u, 0u, 0u, 0u};
        }
    }
    BARRIER();
    STAMP(1, 4);
}

template <int CH> DI void k1_copy_out(const char* smem, char* dst, unsigned stride, int wid, int lane) {
    lds_cptr src = (lds_cptr)smem + wid * CH + lane * 16;
    char* d = dst + (size_t)wid * stride + lane * 16;
    bf16x8 v[5];
#pragma unroll
    for (int i = 0; i < 5; ++i) v[i] = lds_b128(src + i * 1024);
#pragma unroll
    for (int i = 0; i < 5; ++i) *(bf16x8*)(d + i * 1024) = v[i];
    if (CH > 5120 && lane < (CH - 5120) / 16) *(bf16x8*)(d + 5120) = lds_b128(src + 5120);
}

extern "C" __global__ __launch_bounds__(512, 2) void proj_kernel(const char* xA, const char* wqkv, const char* xB, const char* wq, const char* wk, const char* wv, char* ws) {
    extern __shared__ __attribute__((aligned(16))) char smem[];
    const int tid = threadIdx.x, lane = tid & 63, wid = (int)rfl(tid >> 6);
    const int xcd = blockIdx.x & 7, idx = blockIdx.x >> 3, blk = xcd & 3, hh = xcd >> 2, grp = idx >> 3;
    const bool isB = grp == 1;
    const int rt = 4 * blk + (idx & 3);
    const int ct = (grp < 2 ? 0 : 4 * (grp - 1)) + 2 * hh + ((idx >> 2) & 1);
    STAMP_DECL;
    const int kind = isB ? 0 : ct >> 2, head0 = 8 * (ct & 3), row0 = rt * 128;
    char* dst = kind == 2 ? ws + VF_OFF + (size_t)head0 * VF_HEAD + (row0 >> 4) * VB : kind == 1 ? ws + KF_OFF + (size_t)head0 * KF_HEAD + (row0 >> 5) * KT : ws + QF_OFF + (size_t)((isB ? 32 : 0) + head0) * HEAD40 + row0 * 40;
    const unsigned dstride = kind == 2 ? VF_HEAD : kind == 1 ? KF_HEAD : HEAD40;
    if (wid >= 4) {
        const char* xg = (isB ? xB : xA) + (size_t)row0 * (DM * 2);
        const int wpitch = isB ? DM * 2 : 3 * DM * 2;
        const char* wg = (isB ? wq : wqkv) + ((ct * 320) & ~127);
        const char* w2g = ct >= 2 ? wv : wk;
        const unsigned lds0 = (unsigned)(uintptr_t)smem;
        const char* tdst = (kind == 0 || (row0 & 128) == 0) ? (const char*)((uintptr_t)dst & ~(uintptr_t)127) : nullptr;
        if (isB) k1_loader<11>(xg, wg, w2g, wpitch, wid - 4, lane, lds0, tdst, dstride); else k1_loader<10>(xg, wg, w2g, wpitch, wid - 4, lane, lds0, tdst, dstride);
        asm volatile("s_barrier" ::: "memory");
        { lds_ptr st = (lds_ptr)smem; const int t = tid - 256;
          if (kind == 1 && t < 32) *(__attribute__((address_space(3))) u32x4*)(st + t * KT + 1280) = u32x4{0u, 0u, 0u, 0u};
          if (kind == 2) {
              if (t < 64) *(__attribute__((address_space(3))) u32x4*)(st + t * VB + 672) = u32x4{0u, 0u, 0u, 0u};
              if (t >= 64 && t < 192) *(__attribute__((address_space(3))) u32x4*)(st + ((t - 64) >> 1) * VB + (t & 1) * 336 + 320) = u32x4{0x3F803F80u, 0x3F803F80u, 0x3F803F80u, 0x3F803F80u};
          } }
        BARRIER();
    } else {
        {
            const int bb = blockIdx.x, j0 = lane & 31, j = j0 < 27 ? j0 : 26, s = 2 + 2 * wid + (lane >> 5);
            const char* p;
            if (j < 16) p = (j < 8 ? xA : xB) + (size_t)(8 * bb + (j & 7)) * (DM * 2) + s * 128;
            else if (j < 24) { int n = 8 * bb + j - 16; n = n < 1920 ? n : 1919; const int r = n / 30; p = wqkv + (size_t)(64 * s + r) * (3 * DM * 2) + (n - 30 * r) * 128; }
            else { int n = 3 * bb + j - 24; n = n < 640 ? n : 639; const int r = n / 10; p = wq + (size_t)(64 * s + r) * (DM * 2) + (n - 10 * r) * 128; }
            touch_addr(p, rfl((unsigned)(uintptr_t)smem + 3 * K1_SLOT));
        }
        const int shift32 = (ct & 1) * 2, xo = (ct & 1) * 12;
        if (isB) { if (ct < 2) k1_consumer<0, 1>(ws, smem, wid, lane, shift32, xo, row0, dst, dstride STP_ARG); else k1_consumer<0, 2>(ws, smem, wid, lane, shift32, xo, row0, dst, dstride STP_ARG); }
        else if (kind == 2) k1_consumer<2, 0>(ws, smem, wid, lane, shift32, 0, row0, dst, dstride STP_ARG);
        else if (kind == 1) k1_consumer<1, 0>(ws, smem, wid, lane, shift32, 0, row0, dst, dstride STP_ARG);
        else k1_consumer<0, 0>(ws, smem, wid, lane, shift32, 0, row0, dst, dstride STP_ARG);
    }
    if (kind == 2) k1_copy_out<8 * VB>(smem, dst, dstride, wid, lane);
    else if (kind == 1) k1_copy_out<4 * KT>(smem, dst, dstride, wid, lane);
    else k1_copy_out<5120>(smem, dst, dstride, wid, lane);
    if (wid < 4) { STAMP(1, 5); DIAG_DRAIN(); STAMP(1, 7); STAMP_DUMP(1); }
}

extern "C" void kernel_launch(void* const* d_in, const int* in_sizes, int n_in, void* d_out, int out_size, void* d_ws, size_t ws_size, hipStream_t stream) {
    (void)in_sizes; (void)n_in; (void)out_size; (void)ws_size;
    const char* xA = (const char*)d_in[0]; const char* wqkv = (const char*)d_in[1]; const char* woA = (const char*)d_in[2];
    const char* xB = (const char*)d_in[3]; const char* wq = (const char*)d_in[4]; const char* wk = (const char*)d_in[5]; const char* wv = (const char*)d_in[6]; const char* woB = (const char*)d_in[7];
    char* ws = (char*)d_ws;
    (void)hipFuncSetAttribute((const void*)proj_kernel, hipFuncAttributeMaxDynamicSharedMemorySize, K1_LDS);
    (void)hipFuncSetAttribute((const void*)attn_kernel, hipFuncAttributeMaxDynamicSharedMemorySize, A_LDS);
    (void)hipFuncSetAttribute((const void*)outproj_kernel, hipFuncAttributeMaxDynamicSharedMemorySize, K3_LDS + 256);
    proj_kernel<<<256, 512, K1_LDS, stream>>>(xA, wqkv, xB, wq, wk, wv, ws);
    attn_kernel<<<256, 1024, A_LDS, stream>>>(ws, woA, woB);
    outproj_kernel<<<240, 512, K3_LDS + 256, stream>>>(ws, woA, woB, (char*)d_out);
}
```

```cpp
#include <hip/hip_runtime.h>
#include <stdint.h>

typedef short bf16x8 __attribute__((ext_vector_type(8)));
typedef short bf16x4 __attribute__((ext_vector_type(4)));
typedef float f32x2 __attribute__((ext_vector_type(2)));
typedef float f32x4 __attribute__((ext_vector_type(4)));
typedef float f32x16 __attribute__((ext_vector_type(16)));
typedef unsigned u32x2 __attribute__((ext_vector_type(2)));
typedef unsigned u32x4 __attribute__((ext_vector_type(4)));
typedef __bf16 bf16x2_t __attribute__((ext_vector_type(2)));
typedef short v4i16_t __attribute__((ext_vector_type(4)));
typedef __attribute__((address_space(3))) const char* lds_cptr;
typedef __attribute__((address_space(3))) char* lds_ptr;

#define DI __device__ __forceinline__
#define MAP2 2

constexpr int SEQ = 2048, DM = 640;
constexpr float QSCALE = 0.22360679774997896f * 1.4426950408889634f;

constexpr unsigned HEAD40 = SEQ * 40, KT = 1296, VB = 688, KF_HEAD = 64 * KT, VF_HEAD = 128 * VB;
constexpr unsigned QF_OFF = 0, KF_OFF = QF_OFF + 64 * HEAD40, VF_OFF = KF_OFF + 33 * KF_HEAD, OF_OFF = VF_OFF + 33 * VF_HEAD + 2048;

DI unsigned cvtpk(float lo, float hi) { f32x2 v = {lo, hi}; bf16x2_t b = __builtin_convertvector(v, bf16x2_t); return __builtin_bit_cast(unsigned, b); }
DI u32x2 pack4(f32x4 a) { return u32x2{cvtpk(a[0], a[1]), cvtpk(a[2], a[3])}; }
DI unsigned rfl(unsigned x) { return __builtin_amdgcn_readfirstlane(x); }
DI void glds16(const char* sbase, unsigned voff, unsigned lds_dst) {
    asm volatile("s_mov_b32 m0, %2\n\ts_nop 0\n\tglobal_load_lds_dwordx4 %0, %1" :: "v"(voff), "s"(sbase), "s"(lds_dst) : "memory");
}
template <int N> DI void wait_vm() { asm volatile("s_waitcnt vmcnt(%0)" :: "n"(N) : "memory"); }
DI bf16x8 lds_b128(lds_cptr p) { return *(const __attribute__((address_space(3))) bf16x8*)p; }
DI bf16x4 lds_b64(lds_cptr p) { return *(const volatile __attribute__((address_space(3))) bf16x4*)p; }
DI bf16x4 lds_tr(lds_cptr p) { return __builtin_bit_cast(bf16x4, __builtin_amdgcn_ds_read_tr16_b64_v4i16((__attribute__((address_space(3))) v4i16_t*)p)); }
DI void lds_st64(lds_ptr p, u32x2 v) { *(__attribute__((address_space(3))) u32x2*)p = v; }
DI bf16x8 cat8(bf16x4 a, bf16x4 b) { return __builtin_shufflevector(a, b, 0, 1, 2, 3, 4, 5, 6, 7); }
DI int swap23(int r) { return (r & ~12) | ((r & 4) << 1) | ((r & 8) >> 1); }
DI void touch_lines(const char* p, unsigned voff, unsigned lds_scratch) {
    asm volatile("s_mov_b32 m0, %2\n\ts_nop 0\n\tglobal_load_lds_dword %0, %1" :: "v"(voff), "s"(p), "s"(lds_scratch) : "memory");
}
DI void touch_addr(const char* p, unsigned lds_scratch) {
    asm volatile("s_mov_b32 m0, %1\n\ts_nop 0\n\tglobal_load_lds_dword %0, off" :: "v"(p), "s"(lds_scratch) : "memory");
}
#define CODE_ADDR(var, sym) asm volatile("s_getpc_b64 s[60:61]\n\ts_add_u32 s60, s60, " sym "@rel32@lo+4\n\ts_addc_u32 s61, s61, " sym "@rel32@hi+12\n\ts_mov_b64 %0, s[60:61]" : "=s"(var) :: "scc", "s60", "s61")
template <int POL> DI void st128(char* p, bf16x8 v) {
    if (POL == 0) *(bf16x8*)p = v;
    else if (POL == 1) asm volatile("global_store_dwordx4 %0, %1, off nt\n\ts_nop 1" :: "v"(p), "v"(v) : "memory");
    else if (POL == 2) asm volatile("global_store_dwordx4 %0, %1, off sc1\n\ts_nop 1" :: "v"(p), "v"(v) : "memory");
    else asm volatile("global_store_dwordx4 %0, %1, off sc0 sc1\n\ts_nop 1" :: "v"(p), "v"(v) : "memory");
}
#define POL1 2
#define POL2 0
#define POL3 2
#define MFMA16(a, b, c) __builtin_amdgcn_mfma_f32_16x16x32_bf16(a, b, c, 0, 0, 0)
#define BARRIER() do { asm volatile("" ::: "memory"); __builtin_amdgcn_s_waitcnt(0xC07F); __builtin_amdgcn_s_barrier(); asm volatile("" ::: "memory"); } while (0)
#define SBAR() __builtin_amdgcn_sched_barrier(0)
#define K1_RPM 1
template <int NM, int NR> DI void interleave() {
#pragma unroll
    for (int i = 0; i < NR / K1_RPM; ++i) { __builtin_amdgcn_sched_group_barrier(0x008, 1, 0); __builtin_amdgcn_sched_group_barrier(0x100, K1_RPM, 0); }
    __builtin_amdgcn_sched_group_barrier(0x008, NM - NR / K1_RPM, 0);
    SBAR();
}
#define STAMP_DECL
#define STAMP(kern, slot)
#define STAMP_DUMP(kern)
#define DIAG_DRAIN()

constexpr int A_PART = 6144, A_SLOT = 4 * A_PART, A_X = 3 * A_SLOT, A_LDS = A_X + 16 * 3072;
static_assert(4 * KT <= A_PART && 8 * VB <= A_PART && A_SLOT == 24576, "stage layout");

#define MFMA32(a, b, c) __builtin_amdgcn_mfma_f32_32x32x16_bf16(a, b, c, 0, 0, 0)
#define MFMA32R(a, b, c) __builtin_amdgcn_mfma_f32_32x32x8bf16_1k(a, b, c, 0, 0, 0)

DI void attn_exact(const char* kf, const char* vf, bf16x8 qm, bf16x4 qr, int lane, f32x16& o) {
    const int r32 = lane & 31, h = lane >> 5;
    float mrun = -3.0e38f;
#pragma unroll
    for (int r = 0; r < 16; ++r) o[r] = 0.f;
#pragma unroll 1
    for (int t = 0; t < 64; ++t) {
        const bf16x8 km = *(const bf16x8*)(kf + t * KT + lane * 16);
        const bf16x4 kr = *(const bf16x4*)(kf + t * KT + 1024 + r32 * 8);
        const int vo = r32 < 21 ? h * 336 + r32 * 16 : 672;
        const bf16x8 v0 = *(const bf16x8*)(vf + (2 * t) * VB + vo);
        const bf16x8 v1 = *(const bf16x8*)(vf + (2 * t + 1) * VB + vo);
        f32x16 s;
#pragma unroll
        for (int r = 0; r < 16; ++r) s[r] = 0.f;
        s = MFMA32(km, qm, s);
        s = MFMA32R(kr, qr, s);
        float mx = s[0];
#pragma unroll
        for (int r = 1; r < 16; ++r) mx = fmaxf(mx, s[r]);
        { auto rr = __builtin_amdgcn_permlane32_swap(__float_as_uint(mx), __float_as_uint(mx), false, false); mx = fmaxf(__uint_as_float(rr[0]), __uint_as_float(rr[1])); }
        const float mnew = fmaxf(mrun, mx), alpha = __builtin_amdgcn_exp2f(mrun - mnew);
        mrun = mnew;
        u32x4 p0, p1;
#pragma unroll
        for (int i = 0; i < 4; ++i) {
            p0[i] = cvtpk(__builtin_amdgcn_exp2f(s[2 * i] - mnew), __builtin_amdgcn_exp2f(s[2 * i + 1] - mnew));
            p1[i] = cvtpk(__builtin_amdgcn_exp2f(s[8 + 2 * i] - mnew), __builtin_amdgcn_exp2f(s[8 + 2 * i + 1] - mnew));
        }
#pragma unroll
        for (int r = 0; r < 16; ++r) o[r] *= alpha;
        o = MFMA32(v0, __builtin_bit_cast(bf16x8, p0), o);
        o = MFMA32(v1, __builtin_bit_cast(bf16x8, p1), o);
    }
}

DI float row_sum_of(const f32x16& o) {
    auto rr = __builtin_amdgcn_permlane32_swap(__float_as_uint(o[8]), __float_as_uint(o[8]), false, false);
    return __uint_as_float(rr[1]);
}

#define R_SA "v[64:79]"
#define R_SB "v[80:95]"
#define R_PL "v[100:103]"
#define R_PH "v[104:107]"
#define R_KM "v[108:111]"
#define R_KR "v[112:113]"
#define R_V0 "v[114:117]"
#define R_V1 "v[118:121]"
#define STR_(x) #x
#define STR(x) STR_(x)
#define MF16(d, a, b, c) "v_mfma_f32_32x32x16_bf16 " d ", " a ", " b ", " c "\n\t"
#define MF8(d, a, b, c) "v_mfma_f32_32x32x8_bf16 " d ", " a ", " b ", " c "\n\t"
#define CVT2(p0, p1) "v_cvt_pk_bf16_f32 v" STR(p0) ", v96, v97\n\tv_cvt_pk_bf16_f32 v" STR(p1) ", v98, v99\n\t"
#define EXP4(s0, s1, s2, s3) "v_exp_f32 v96, v" STR(s0) "\n\tv_exp_f32 v97, v" STR(s1) "\n\tv_exp_f32 v98, v" STR(s2) "\n\tv_exp_f32 v99, v" STR(s3) "\n\t"
#define GAP(p0, p1, s0, s1, s2, s3) CVT2(p0, p1) EXP4(s0, s1, s2, s3)
#define WAITL(n) "s_waitcnt lgkmcnt(" STR(n) ")\n\t"
#define RD128(d, a, off) "ds_read_b128 " d ", " a " offset:" STR(off) "\n\t"
#define RD64(d, a, off) "ds_read_b64 " d ", " a " offset:" STR(off) "\n\t"
#define TILE(MID, KOFF, V1OFF, V0OFF) \
    MF16(R_SB, R_KM, "%[qbm]", "0") RD128(R_KM, "v45", KOFF) GAP(106, 107, 64, 65, 66, 67) \
    MF8(R_SB, R_KR, "%[qbr]", R_SB) RD64(R_KR, "v46", KOFF) GAP(100, 101, 68, 69, 70, 71) \
    MF16("%[ob]", R_V1, R_PH, "%[ob]") RD128(R_V1, "v47", V1OFF) GAP(102, 103, 72, 73, 74, 75) \
    WAITL(3) MF16("%[oa]", R_V0, R_PL, "%[oa]") GAP(104, 105, 76, 77, 78, 79) \
    MID \
    WAITL(2) MF16(R_SA, R_KM, "%[qam]", "0") GAP(106, 107, 80, 81, 82, 83) \
    WAITL(1) MF8(R_SA, R_KR, "%[qar]", R_SA) GAP(100, 101, 84, 85, 86, 87) \
    WAITL(0) MF16("%[oa]", R_V1, R_PH, "%[oa]") GAP(102, 103, 88, 89, 90, 91) \
    MF16("%[ob]", R_V0, R_PL, "%[ob]") RD128(R_V0, "v47", V0OFF) GAP(104, 105, 92, 93, 94, 95)
#define TILE_LAST(MID, PRE_K, V1OFF, PRE_V0) \
    MF16(R_SB, R_KM, "%[qbm]", "0") PRE_K RD128(R_KM, "v45", 0) GAP(106, 107, 64, 65, 66, 67) \
    MF8(R_SB, R_KR, "%[qbr]", R_SB) RD64(R_KR, "v46", 0) GAP(100, 101, 68, 69, 70, 71) \
    MF16("%[ob]", R_V1, R_PH, "%[ob]") RD128(R_V1, "v47", V1OFF) GAP(102, 103, 72, 73, 74, 75) \
    WAITL(3) MF16("%[oa]", R_V0, R_PL, "%[oa]") GAP(104, 105, 76, 77, 78, 79) \
    MID \
    WAITL(2) MF16(R_SA, R_KM, "%[qam]", "0") GAP(106, 107, 80, 81, 82, 83) \
    WAITL(1) MF8(R_SA, R_KR, "%[qar]", R_SA) GAP(100, 101, 84, 85, 86, 87) \
    WAITL(0) MF16("%[oa]", R_V1, R_PH, "%[oa]") GAP(102, 103, 88, 89, 90, 91) \
    MF16("%[ob]", R_V0, R_PL, "%[ob]") PRE_V0 RD128(R_V0, "v47", 0) GAP(104, 105, 92, 93, 94, 95)

#define P0 2
#define P1 2
#define P2 1
#define P3 1
#define P4 0
#define P5 0
#define P6 3
#define P7 3
#define PRIO(n) "s_setprio " STR(n) "\n\t"
#define ASM_STAMP(r)
#define TOUCH_PF "global_load_lds_dword v42, %[pf]\n\t"
#define TOUCH_PO "global_load_lds_dword v41, %[po]\n\t"
#define DMA2(SLOTREG) \
    "s_cmp_ge_u32 %[wid], 8\n\ts_cbranch_scc1 5f\n\t" \
    "s_lshr_b32 s46, s45, 1\n\ts_add_u32 s46, s46, %[blk]\n\ts_and_b32 s46, s46, 3\n\ts_lshl_b32 s46, s46, 2\n\t" \
    "s_and_b32 s47, s45, 1\n\ts_add_u32 s46, s46, s47\n\ts_lshl_b32 s46, s46, 2\n\ts_add_u32 s45, s45, 1\n\t" \
    "s_mul_i32 s47, s46, 1296\n\ts_add_u32 s60, s48, s47\n\ts_addc_u32 s61, s49, 0\n\t" \
    "s_add_u32 m0, %[d0], " SLOTREG "\n\ts_mul_i32 s47, s46, %[p1]\n\tglobal_load_lds_dwordx4 v40, s[60:61]\n\t" \
    "s_add_u32 s60, s50, s47\n\ts_addc_u32 s61, s51, 0\n\t" \
    "s_add_u32 m0, m0, 0x2000\n\ts_mul_i32 s47, s46, 1376\n\tglobal_load_lds_dwordx4 v40, s[60:61]\n\t" \
    "s_add_u32 s60, s54, s47\n\ts_addc_u32 s61, s55, 0\n\t" \
    "s_add_u32 m0, m0, 0x2000\n\ts_nop 0\n\tglobal_load_lds_dwordx4 v40, s[60:61]\n\t" \
    "5:\n\t"

extern "C" __global__ __launch_bounds__(1024, 4) void attn_kernel(char* ws, const char* woA, const char* woB) {
    extern __shared__ __attribute__((aligned(16))) char smem[];
    const int tid = threadIdx.x, lane = tid & 63, wid = (int)rfl(tid >> 6);
    const int xcd = blockIdx.x & 7, idx = blockIdx.x >> 3;
    const int qblk = 2 * (xcd & 1) + (idx & 1), branch = idx >> 4, head = 8 * (xcd >> 1) + ((idx >> 1) & 7), kblk = 2 * (xcd & 1);
    const int kvh = branch ? 32 : head;
    const int qg = wid & 7, kh = wid >> 3;
    const unsigned kf_off = KF_OFF + kvh * KF_HEAD, vf_off = VF_OFF + kvh * VF_HEAD;
    const unsigned lds0 = (unsigned)(uintptr_t)smem;
    STAMP_DECL;
    STAMP(2, 0);
    const unsigned w8 = wid & 7, w1k = w8 * 1024;
    const unsigned src0 = kf_off + w1k + (w8 >= 6 ? 8 * KT - 6144 : 0);
    const unsigned src1 = w8 < 4 ? kf_off + 8 * KT + 2048 + w1k : vf_off + w1k - 4096, pitch1 = w8 < 4 ? KT : 2 * VB;
    const unsigned src2 = vf_off + w1k + (w8 < 2 ? 4096 : 16 * VB - 2048);
    const unsigned d0 = lds0 + wid * 1024;
    if (wid < 8) {
        const unsigned t0 = kblk * 16, l16 = lane * 16;
        glds16(ws + (src0 + t0 * KT), l16, d0); glds16(ws + (src1 + t0 * pitch1), l16, d0 + 8192); glds16(ws + (src2 + t0 * 2 * VB), l16, d0 + 16384);
    }
    __builtin_amdgcn_sched_barrier(0);
    const int qrow0 = qblk * 512 + qg * 64;
    const unsigned qoff = rfl(QF_OFF + (unsigned)(branch * 32 + head) * HEAD40 + (unsigned)qrow0 * 40);
    const unsigned kbase = rfl(lds0 + kh * A_PART), vbase = rfl(lds0 + 2 * A_PART + kh * A_PART);
    const char* pf; unsigned pfmax;
    { const char* code; CODE_ADDR(code, "outproj_kernel");
      pf = wid < 4 ? (xcd >> 2 ? woB : woA) + (idx * 200 + wid * 50) * 128 : code + (wid == 5 ? 8192 : 0); pfmax = wid < 4 ? 49 : 63; }
    const char* po = ws + OF_OFF + (size_t)((unsigned)(branch * 32 + head) * HEAD40 + (unsigned)(qrow0 + 32 * kh) * 40);
    bf16x8 qam, qbm; bf16x4 qar, qbr;
    f32x16 oa, ob;
    asm volatile(
        ASM_STAMP("s[72:73]")
        "v_lshlrev_b32 v40, 4, %[lane]\n\t"
        "s_mov_b64 s[52:53], %[ws]\n\t"
        "s_add_u32 s48, s52, %[src0]\n\ts_addc_u32 s49, s53, 0\n\ts_add_u32 s50, s52, %[src1]\n\ts_addc_u32 s51, s53, 0\n\ts_add_u32 s54, s52, %[src2]\n\ts_addc_u32 s55, s53, 0\n\t"
        "s_mov_b32 s45, 1\n\t"
        "v_and_b32 v41, 31, %[lane]\n\tv_lshrrev_b32 v42, 5, %[lane]\n\t"
        "s_add_u32 s62, s52, %[qoff]\n\ts_addc_u32 s63, s53, 0\n\t"
        "v_lshlrev_b32 v43, 3, v41\n\t"
        "v_mov_b64 %[qar], 0\n\tv_mov_b64 %[qbr], 0\n\t"
        "global_load_dwordx4 %[qam], v40, s[62:63]\n\tglobal_load_dwordx4 %[qbm], v40, s[62:63] offset:1280\n\t"
        "s_mov_b32 exec_hi, 0\n\t"
        "global_load_dwordx2 %[qar], v43, s[62:63] offset:1024\n\tglobal_load_dwordx2 %[qbr], v43, s[62:63] offset:2304\n\t"
        "s_mov_b32 exec_hi, -1\n\t"
        "v_cmp_eq_u32 vcc, 0, v42\n\t"
        "v_add_u32 v45, %[kbase], v40\n\t"
        "v_add_u32 v46, 0x400, v43\n\tv_mov_b32 v48, 0x500\n\ts_nop 0\n\t"
        "v_cndmask_b32 v46, v48, v46, vcc\n\tv_add_u32 v46, %[kbase], v46\n\t"
        "v_cmp_gt_u32 vcc, 21, v41\n\t"
        "v_mul_u32_u24 v47, 0x150, v42\n\tv_lshl_add_u32 v47, v41, 4, v47\n\tv_mov_b32 v48, 0x2a0\n\t"
        "v_cndmask_b32 v47, v48, v47, vcc\n\tv_add_u32 v47, %[vbase], v47\n\t"
        "v_mov_b32 v118, 0\n\tv_mov_b32 v119, 0\n\tv_mov_b32 v120, 0\n\tv_mov_b32 v121, 0\n\t"
        "v_mov_b32 v96, 0\n\tv_mov_b32 v97, 0\n\tv_mov_b32 v98, 0\n\tv_mov_b32 v99, 0\n\t"
        "v_mov_b32 v104, 0\n\tv_mov_b32 v105, 0\n\t"
        "v_mfma_f32_32x32x16_bf16 %[oa], v[118:121], v[118:121], 0\n\tv_mfma_f32_32x32x16_bf16 %[ob], v[118:121], v[118:121], 0\n\t"
        "s_mov_b32 s40, 0\n\t"
        "s_mov_b32 s41, 0\n\t"
        "s_mov_b32 s44, 24576\n\t"
        "s_waitcnt vmcnt(4)\n\t" ASM_STAMP("s[66:67]") "s_barrier\n\t" ASM_STAMP("s[68:69]")
        RD128(R_KM, "v45", 0) RD64(R_KR, "v46", 0) RD128(R_V0, "v47", 0)
        "s_waitcnt vmcnt(0)\n\t"
        DMA2("s44")
        WAITL(0)
        MF16(R_SA, R_KM, "%[qam]", "0") MF8(R_SA, R_KR, "%[qar]", R_SA)
        "s_nop 15\n\ts_nop 3\n\t"
        ASM_STAMP("s[70:71]")
        "2:\n\t"
        "s_add_u32 s42, s41, 24576\n\ts_cmp_eq_u32 s42, 73728\n\ts_cselect_b32 s42, 0, s42\n\t"
        "s_sub_u32 s43, s42, s41\n\t"
        "s_add_u32 s44, s42, 24576\n\ts_cmp_eq_u32 s44, 73728\n\ts_cselect_b32 s44, 0, s44\n\t"
        PRIO(P0) TILE(PRIO(P1), 1296, 688, 1376)
        PRIO(P2) TILE(PRIO(P3), 2592, 2064, 2752)
        PRIO(P4) TILE(PRIO(P5), 3888, 3440, 4128)
        PRIO(P6) TILE_LAST(PRIO(P7),
            "s_waitcnt vmcnt(0)\n\ts_barrier\n\t"
            "v_add_u32 v45, s43, v45\n\tv_add_u32 v46, s43, v46\n\t"
            "s_cmp_ge_u32 s40, 6\n\ts_cbranch_scc1 3f\n\t"
            DMA2("s44")
            "s_branch 4f\n\t"
            "3:\n\t"
            "s_cmp_lg_u32 s40, 6\n\ts_cbranch_scc1 4f\n\t"
            "v_lshrrev_b32 v41, 4, v40\n\tv_min_u32 v42, %[pfmax], v41\n\tv_min_u32 v41, 9, v41\n\tv_lshlrev_b32 v42, 7, v42\n\tv_lshlrev_b32 v41, 7, v41\n\t"
            "s_add_u32 m0, %[d0], s44\n\ts_nop 0\n\t"
            TOUCH_PF TOUCH_PO
            "4:\n\t",
            4816, "v_add_u32 v47, s43, v47\n\t")
        "s_mov_b32 s41, s42\n\t"
        "s_add_u32 s40, s40, 1\n\t"
        "s_cmp_lt_u32 s40, 8\n\ts_cbranch_scc1 2b\n\t"
        "s_nop 1\n\t" CVT2(106, 107) "s_nop 1\n\t"
        MF16("%[ob]", R_V1, R_PH, "%[ob]")
        "s_waitcnt lgkmcnt(0)\n\ts_nop 15\n\ts_nop 3\n\t"
        : [oa] "=&v"(oa), [ob] "=&v"(ob), [qam] "=&v"(qam), [qbm] "=&v"(qbm), [qar] "=&v"(qar), [qbr] "=&v"(qbr)
        : [lane] "v"(lane), [ws] "s"(ws), [src0] "s"(src0), [src1] "s"(src1), [src2] "s"(src2), [p1] "s"(pitch1), [blk] "s"(kblk), [qoff] "s"(qoff),
          [kbase] "s"(kbase), [vbase] "s"(vbase), [d0] "s"(d0), [wid] "s"(wid), [pf] "s"(pf), [po] "s"(po), [pfmax] "s"(pfmax)
        : "memory", "scc", "vcc", "s40", "s41", "s42", "s43", "s44", "s45", "s46", "s47", "s48", "s49", "s50", "s51", "s52", "s53", "s54", "s55", "s60", "s61", "s62", "s63",
          "s64", "s65", "s66", "s67", "s68", "s69", "s70", "s71", "s72", "s73",
          "v40", "v41", "v42", "v43", "v44", "v45", "v46", "v47", "v48",
          "v64", "v65", "v66", "v67", "v68", "v69", "v70", "v71", "v72", "v73", "v74", "v75", "v76", "v77", "v78", "v79",
          "v80", "v81", "v82", "v83", "v84", "v85", "v86", "v87", "v88", "v89", "v90", "v91", "v92", "v93", "v94", "v95",
          "v96", "v97", "v98", "v99", "v100", "v101", "v102", "v103", "v104", "v105", "v106", "v107", "v108", "v109", "v110", "v111",
          "v112", "v113", "v114", "v115", "v116", "v117", "v118", "v119", "v120", "v121");
    STAMP(2, 2);
    const int r32 = lane & 31, h = lane >> 5;
    f32x16 o;
    {
        lds_ptr xb = (lds_ptr)smem + A_X + wid * 3072 + lane * 16;
        if (kh) {
#pragma unroll
            for (int i = 0; i < 3; ++i) *(__attribute__((address_space(3))) f32x4*)(xb + i * 1024) = f32x4{oa[4 * i], oa[4 * i + 1], oa[4 * i + 2], oa[4 * i + 3]};
            o = ob;
        } else {
#pragma unroll
            for (int i = 0; i < 3; ++i) *(__attribute__((address_space(3))) f32x4*)(xb + i * 1024) = f32x4{ob[4 * i], ob[4 * i + 1], ob[4 * i + 2], ob[4 * i + 3]};
            o = oa;
        }
    }
    BARRIER();
    {
        lds_cptr xb = (lds_cptr)smem + A_X + (wid ^ 8) * 3072 + lane * 16;
#pragma unroll
        for (int i = 0; i < 3; ++i) { const f32x4 v = *(const __attribute__((address_space(3))) f32x4*)(xb + i * 1024);
#pragma unroll
            for (int e = 0; e < 4; ++e) o[4 * i + e] += v[e]; }
    }
    float l = row_sum_of(o);
    float mag = 0.f;
#pragma unroll
    for (int r = 0; r < 12; ++r) mag += fabsf(o[r]);
    const bool ok = l >= 0x1p-60f && l <= 0x1p60f && mag <= 3.0e38f;
    if (__builtin_expect(!__all(ok), 0)) {
        attn_exact(ws + kf_off, ws + vf_off, kh ? qbm : qam, kh ? qbr : qar, lane, o);
        l = row_sum_of(o);
    }
    const float inv = __builtin_amdgcn_rcpf(l);
    {
        lds_ptr so = (lds_ptr)smem + A_X + (wid ^ 8) * 3072;
        lds_st64(so + r32 * 40 + 8 * h, u32x2{cvtpk(o[0] * inv, o[1] * inv), cvtpk(o[2] * inv, o[3] * inv)});
        lds_st64(so + r32 * 40 + 16 + 8 * h, u32x2{cvtpk(o[4] * inv, o[5] * inv), cvtpk(o[6] * inv, o[7] * inv)});
        if (h == 0) lds_st64(so + r32 * 40 + 32, u32x2{cvtpk(o[8] * inv, o[9] * inv), cvtpk(o[10] * inv, o[11] * inv)});
        asm volatile("" ::: "memory"); __builtin_amdgcn_s_waitcnt(0xC07F); asm volatile("" ::: "memory");
        char* od = ws + OF_OFF + (size_t)((unsigned)(branch * 32 + head) * HEAD40 + (unsigned)(qrow0 + 32 * kh) * 40) + lane * 16;
        st128<POL2>(od, lds_b128((lds_cptr)so + lane * 16));
        if (lane < 16) st128<POL2>(od + 1024, lds_b128((lds_cptr)so + 1024 + lane * 16));
    }
    DIAG_DRAIN();
    STAMP(2, 7);
    STAMP_DUMP(2);
}

#define K3_RPM 2
constexpr int K3_X = 0, K3_W = 32 * 3840, K3_LDS = K3_W + 4 * 8192;
constexpr int k3_total(int t) { int n = 14; for (int u = 0; u < t; ++u) n += (u <= 16 ? 2 : 0) + (u <= 10 ? 2 : 0); return n; }
constexpr int k3_allowed(int t) {
    int last = t <= 2 ? 10 + 2 * t : k3_total(t - 3) + 2;
    const int xg = t == 5 ? 3 : t == 10 ? 7 : t == 15 ? 10 : -1;
    if (xg >= 0) { const int e = k3_total(xg) + 4; if (e > last) last = e; }
    return k3_total(t) - last;
}
DI int k3_krow(int c, int e) { const int P = c / 5, r = c - 5 * P; return r < 4 ? (2 * P + (r >> 1)) * 20 + (r & 1) * 8 + e : (2 * P + (e >> 2)) * 20 + 16 + (e & 3); }

DI unsigned k3_xoff(int o, int rot) { const int hd = o / 3840; return (unsigned)(((hd + 8 * rot) & 31) * HEAD40 + (o - hd * 3840)); }
template <int T> DI void k3_load_steps(const char* xsrc, const char* wsrc, const unsigned (&wo)[5][2], int lw, int lane, unsigned lds0, int rot) {
    if constexpr (T < 20) {
        wait_vm<k3_allowed(T)>();
        asm volatile("s_barrier" ::: "memory");
        if constexpr (T + 3 < 20) {
            constexpr int t = T + 3;
#pragma unroll
            for (int i = 0; i < 2; ++i) glds16(wsrc + (size_t)((t / 5 + rot) & 3) * 160 * (DM * 2), wo[t % 5][i], lds0 + K3_W + (t & 3) * 8192 + (2 * lw + i) * 1024);
        }
        if constexpr (T <= 10) {
#pragma unroll
            for (int i = 0; i < 2; ++i) { const int pc = lw + 4 * (8 + 2 * T + i); glds16(xsrc, k3_xoff(pc * 1024 + lane * 16, rot), lds0 + K3_X + pc * 1024); }
        }
        k3_load_steps<T + 1>(xsrc, wsrc, wo, lw, lane, lds0, rot);
    }
}

extern "C" __global__ __launch_bounds__(512, 2) void outproj_kernel(char* ws, const char* woA, const char* woB, char* out) {
    extern __shared__ __attribute__((aligned(16))) char smem[];
    const int tid = threadIdx.x, lane = tid & 63, wid = (int)rfl(tid >> 6);
    const int xcd = blockIdx.x & 7, idx = blockIdx.x >> 3, br = xcd >> 2;
    const int blk = 2 * (xcd & 1) + ((xcd >> 1) & 1), rot = xcd >> 1;
    if (idx >= 30) return;
    STAMP_DECL;
    const int rl = idx / 5, ct = idx - 5 * rl, row0 = blk * 512 + (rl < 5 ? rl * 96 : 416);
    if (wid >= 4) {
        const int lw = wid - 4;
        const char* xsrc = ws + OF_OFF + (size_t)br * 32 * HEAD40 + row0 * 40;
        const char* wsrc = (br ? woB : woA) + ct * 256;
        const unsigned lds0 = (unsigned)(uintptr_t)smem;
#pragma unroll
        for (int j = 0; j < 8; ++j) { const int pc = lw + 4 * j; glds16(xsrc, k3_xoff(pc * 1024 + lane * 16, rot), lds0 + K3_X + pc * 1024); }
        SBAR();
        unsigned wo[5][2];
#pragma unroll
        for (int tt = 0; tt < 5; ++tt)
#pragma unroll
            for (int i = 0; i < 2; ++i) {
                const int rho = 4 * (2 * lw + i) + (lane >> 4), s16 = lane & 15, kap = swap23(rho);
                wo[tt][i] = (unsigned)(k3_krow(4 * tt + (kap >> 3), kap & 7) * (DM * 2) + (((s16 >> 1) ^ (rho & 7)) << 5) + (s16 & 1) * 16);
            }
#pragma unroll
        for (int t = 0; t < 3; ++t)
#pragma unroll
            for (int i = 0; i < 2; ++i) glds16(wsrc + (size_t)rot * 160 * (DM * 2), wo[t][i], lds0 + K3_W + t * 8192 + (2 * lw + i) * 1024);
        k3_load_steps<0>(xsrc, wsrc, wo, lw, lane, lds0, rot);
        asm volatile("s_barrier\n\ts_barrier" ::: "memory");
    } else {
        lds_cptr lds = (lds_cptr)smem;
        const int wm = wid >> 1, wn = wid & 1, m = lane & 15, g = lane >> 4, q = (lane & 15) >> 2, p = lane & 3;
        unsigned xlo[5], xhi[5], wa[4];
#pragma unroll
        for (int tt = 0; tt < 5; ++tt) {
            const int c = 4 * tt + g, P = c / 5, r = c - 5 * P, rb = (48 * wm + m) * 40;
            xlo[tt] = (unsigned)(K3_X + rb + (r < 4 ? (2 * P + (r >> 1)) * 3840 + (r & 1) * 16 : 2 * P * 3840 + 32));
            xhi[tt] = (unsigned)(K3_X + rb + (r < 4 ? (2 * P + (r >> 1)) * 3840 + (r & 1) * 16 + 8 : (2 * P + 1) * 3840 + 32));
        }
        { const int sx = (g & 1) * 4 + q;
#pragma unroll
          for (int j = 0; j < 4; ++j) wa[j] = (unsigned)(K3_W + ((g >> 1) * 16 + sx) * 256 + (((4 * wn + j) ^ sx) << 5) + p * 8); }
        f32x4 acc[3][4];
#pragma unroll
        for (int i = 0; i < 3; ++i)
#pragma unroll
            for (int j = 0; j < 4; ++j) acc[i][j] = f32x4{0.f, 0.f, 0.f, 0.f};
        bf16x8 xf[2][3], wf[2][4];
        auto load = [&](int t) {
            const int tt = t % 5, u = t / 5;
#pragma unroll
            for (int i = 0; i < 3; ++i) xf[t & 1][i] = cat8(lds_b64(lds + xlo[tt] + u * 30720 + i * 640), lds_b64(lds + xhi[tt] + u * 30720 + i * 640));
#pragma unroll
            for (int j = 0; j < 4; ++j) wf[t & 1][j] = cat8(lds_tr(lds + wa[j] + (t & 3) * 8192), lds_tr(lds + wa[j] + (t & 3) * 8192 + 2048));
        };
        STAMP(3, 0);
        BARRIER();
        STAMP(3, 1);
        load(0);
        SBAR();
#pragma unroll
        for (int t = 0; t < 20; ++t) {
            if (t + 1 < 20) { BARRIER(); load(t + 1); }
#pragma unroll
            for (int i = 0; i < 3; ++i)
#pragma unroll
                for (int j = 0; j < 4; ++j) acc[i][j] = MFMA16(wf[t & 1][j], xf[t & 1][i], acc[i][j]);
            if (t + 1 < 20) {
#pragma unroll
                for (int i = 0; i < 14 / K3_RPM; ++i) { __builtin_amdgcn_sched_group_barrier(0x100, K3_RPM, 0); __builtin_amdgcn_sched_group_barrier(0x008, 1, 0); }
                __builtin_amdgcn_sched_group_barrier(0x100, 14, 0); __builtin_amdgcn_sched_group_barrier(0x008, 12, 0);
            }
            SBAR();
        }
        STAMP(3, 2);
        BARRIER();
        STAMP(3, 3);
        lds_ptr st = (lds_ptr)smem;
#pragma unroll
        for (int i = 0; i < 3; ++i) {
            const int row = 48 * wm + 16 * i + m;
#pragma unroll
            for (int j = 0; j < 4; ++j) { const int b = (64 * wn + 16 * j + 4 * g) * 2; lds_st64(st + row * 256 + ((((b >> 4) ^ (row & 15)) << 4) | (b & 8)), pack4(acc[i][j])); }
        }
        BARRIER();
        STAMP(3, 4);
    }
#pragma unroll
    for (int it = 0; it < 3; ++it) {
        const int o = it * 8192 + tid * 16, row = o >> 8, s = (o >> 4) & 15;
        st128<POL3>(out + (size_t)(row0 + row) * 2560 + br * 1280 + ct * 256 + ((s ^ (row & 15)) << 4), lds_b128((lds_cptr)smem + o));
    }
    if (wid < 4) { STAMP(3, 5); DIAG_DRAIN(); STAMP(3, 7); STAMP_DUMP(3); }
}

constexpr int NSTAGE = DM / 64;
constexpr int K1_X = 0, K1_W = 16384, K1_W2 = K1_W + 24576, K1_SLOT = K1_W2 + 3072, K1_LDS = 3 * K1_SLOT + 256;

template <int NP> DI void k1_loader(const char* xg, const char* wg, const char* w2g, int wpitch, int lw, int lane, unsigned lds0, const char* tdst, unsigned dstride) {
    unsigned xo[4], wo[6], w2o = 0;
#pragma unroll
    for (int i = 0; i < 4; ++i) { const int row = (4 * lw + i) * 8 + (lane >> 3), pos = lane & 7; xo[i] = (unsigned)(row * (DM * 2) + ((pos ^ ((row >> 1) & 7)) << 4)); }
#pragma unroll
    for (int i = 0; i < 4; ++i) glds16(xg, xo[i], lds0 + K1_X + (4 * lw + i) * 1024);
    SBAR();
#pragma unroll
    for (int j = 0; j < 3; ++j) {
        const int o = j * 1024 + lane * 16, r = (o * 171) >> 16, b = o - r * 384;
        wo[j] = (unsigned)((16 * lw + 8 * (r >> 2) + (r & 3)) * wpitch + (((b >> 5) ^ ((r >> 1) & 3)) << 5) + (b & 16));
        wo[j + 3] = wo[j] + 4u * (unsigned)wpitch;
    }
    const int p2 = lw < 3 ? lw : 2;
    if (NP == 11) { const int o = p2 * 1024 + lane * 16; w2o = (unsigned)(o < 2560 ? o : 2544); }
    auto issue_w = [&](int s, int slot) {
        const unsigned sl = lds0 + (unsigned)slot * K1_SLOT;
        const char* wsrc = wg + (size_t)s * 64 * wpitch;
#pragma unroll
        for (int i = 0; i < 6; ++i) glds16(wsrc, wo[i], sl + K1_W + (6 * lw + i) * 1024);
        if (NP == 11) glds16(w2g + (size_t)s * 2560, w2o, sl + K1_W2 + p2 * 1024);
    };
    auto issue = [&](int s, int slot) {
        const unsigned sl = lds0 + (unsigned)slot * K1_SLOT;
        const char* xs = xg + (size_t)s * 128;
#pragma unroll
        for (int i = 0; i < 4; ++i) glds16(xs, xo[i], sl + K1_X + (4 * lw + i) * 1024);
        issue_w(s, slot);
    };
    issue_w(0, 0); issue(1, 1);
    int nslot = 2;
#pragma unroll 1
    for (int s = 0; s < NSTAGE; ++s) {
        if (s < NSTAGE - 1) wait_vm<NP>(); else wait_vm<0>();
        asm volatile("s_barrier" ::: "memory");
        if (s + 2 < NSTAGE) { issue(s + 2, nslot); nslot = nslot == 2 ? 0 : nslot + 1; }
    }
}

#define TOUCH_ALL 0
struct K1Frag { bf16x8 x[4]; bf16x8 w[5]; bf16x8 w2; };

#define STP_PARAM
#define STP_ARG
template <int KIND, int XTRA> DI void k1_consumer(char* ws, char* smem, int wid, int lane, int shift32, int xo, int row0, const char* dst, unsigned dstride STP_PARAM) {
    lds_cptr lds = (lds_cptr)smem;
    const int wm = wid >> 1, wn = wid & 1, m = lane & 15, g = lane >> 4, q = (lane & 15) >> 2, p = lane & 3;
    const unsigned xa0 = K1_X + (unsigned)((64 * wm + m) * 128 + ((g ^ ((m >> 1) & 7)) << 4));
    unsigned wa[5];
    { const int gx = ((g & 1) << 1) | (q >> 1);
#pragma unroll
      for (int j = 0; j < 5; ++j) wa[j] = K1_W + (unsigned)(((g >> 1) * 16 + (g & 1) * 4 + q) * 384 + (((shift32 + 5 * wn + j) ^ gx) << 5) + p * 8); }
    const unsigned w2a = K1_W2 + (unsigned)((8 * g + q) * 40 + p * 8 + xo * 2);
    f32x4 acc[4][5], acc2[2];
#pragma unroll
    for (int i = 0; i < 4; ++i)
#pragma unroll
        for (int j = 0; j < 5; ++j) acc[i][j] = f32x4{0.f, 0.f, 0.f, 0.f};
    acc2[0] = acc2[1] = f32x4{0.f, 0.f, 0.f, 0.f};
    auto load = [&](K1Frag& f, unsigned sl, int kk) {
        lds_cptr b = lds + sl;
#pragma unroll
        for (int i = 0; i < 4; ++i) f.x[i] = lds_b128(b + ((xa0 ^ (kk * 64)) + i * 2048));
#pragma unroll
        for (int j = 0; j < 5; ++j) f.w[j] = cat8(lds_tr(b + wa[j] + kk * 12288), lds_tr(b + wa[j] + kk * 12288 + 3072));
        if (XTRA) f.w2 = cat8(lds_tr(b + w2a + kk * 1280), lds_tr(b + w2a + kk * 1280 + 160));
    };
    auto mma = [&](const K1Frag& f) {
#pragma unroll
        for (int i = 0; i < 4; ++i)
#pragma unroll
            for (int j = 0; j < 5; ++j) acc[i][j] = KIND == 2 ? MFMA16(f.x[i], f.w[j], acc[i][j]) : MFMA16(f.w[j], f.x[i], acc[i][j]);
        if (XTRA) {
#pragma unroll
            for (int i = 0; i < 2; ++i) { const bf16x8 xx = wn ? f.x[2 + i] : f.x[i]; acc2[i] = XTRA == 2 ? MFMA16(xx, f.w2, acc2[i]) : MFMA16(f.w2, xx, acc2[i]); }
        }
    };
    constexpr int CH = KIND == 0 ? 5120 : KIND == 1 ? 4 * KT : 8 * VB, TP = KIND == 0 ? 1280 : KT;
    unsigned sa[5], sh[5];
#pragma unroll
    for (int j = 0; j < 5; ++j) {
        const int col = 80 * wn + 16 * j + (KIND == 2 ? m : 4 * g), hl = (col * 205) >> 12, d = col - hl * 20;
        if (KIND == 2) { sa[j] = (unsigned)(hl * CH + 4 * wm * VB + (g & 1) * 336 + (g >> 1) * 8 + d * 16); sh[j] = 0; }
        else { const bool rem = d == 16; sa[j] = (unsigned)(hl * CH + 2 * wm * TP + (rem ? 1024 + m * 8 : (d >> 3) * 512 + m * 16 + (d & 4) * 2)); sh[j] = sa[j] + (rem ? 128 : 256); asm volatile("" : "+v"(sh[j])); }
        asm volatile("" : "+v"(sa[j]));
    }
    K1Frag f0, f1;
    STAMP(1, 0);
    BARRIER();
    STAMP(1, 1);
    const unsigned scratch = rfl((unsigned)(uintptr_t)smem + 3 * K1_SLOT);
    load(f0, 0, 0);
    int slot = 0;
    constexpr int NM = XTRA ? 22 : 20, NR = XTRA ? 16 : 14;
    SBAR();
#pragma unroll 1
    for (int s = 0; s < NSTAGE - 1; ++s) {
        load(f1, (unsigned)slot * K1_SLOT, 1);
        mma(f0);
        interleave<NM, NR>();
        slot = slot == 2 ? 0 : slot + 1;
        BARRIER();
        if (wid == (s & 3)) {
            if (s == 8) { const char* code; CODE_ADDR(code, "attn_kernel"); touch_lines(code, lane * 128, scratch); }
            SBAR();
        }
        load(f0, (unsigned)slot * K1_SLOT, 0);
        mma(f1);
        interleave<NM, NR>();
    }
    load(f1, (unsigned)slot * K1_SLOT, 1);
    mma(f0);
    interleave<NM, NR>();
    mma(f1);
    STAMP(1, 2);
    BARRIER();
    STAMP(1, 3);
    lds_ptr st = (lds_ptr)smem;
#pragma unroll
    for (int j = 0; j < 5; ++j) {
        if (KIND == 2) {
#pragma unroll
            for (int i = 0; i < 4; ++i) lds_st64(st + sa[j] + i * VB, pack4(acc[i][j]));
        } else {
            const float sc = KIND == 0 ? QSCALE : 1.0f;
            lds_st64(st + sa[j], pack4(acc[0][j] * sc)); lds_st64(st + sa[j] + TP, pack4(acc[2][j] * sc));
            lds_st64(st + sh[j], pack4(acc[1][j] * sc)); lds_st64(st + sh[j] + TP, pack4(acc[3][j] * sc));
        }
    }
    if (XTRA == 1) {
        const int d0 = xo + 4 * g;
        if (d0 < (xo ? 20 : 12)) {
#pragma unroll
            for (int i = 0; i < 2; ++i) {
                const int row = row0 + 64 * wm + 32 * wn + 16 * i + m, key = row & 31;
                *(u32x2*)(ws + KF_OFF + (size_t)32 * KF_HEAD + (row >> 5) * KT + (d0 < 16 ? (d0 >> 3) * 512 + key * 16 + (d0 & 4) * 2 : 1024 + key * 8)) = pack4(acc2[i]);
            }
        }
        if (xo == 0 && lane == 0 && wn == 0) {
#pragma unroll
            for (int i = 0; i < 2; ++i) *(u32x4*)(ws + KF_OFF + (size_t)32 * KF_HEAD + ((row0 >> 5) + 2 * wm + i) * KT + 1280) = u32x4{0u, 0u, 0u, 0u};
        }
    }
    if (XTRA == 2) {
        const int d = xo + m;
#pragma unroll
        for (int i = 0; i < 2; ++i) {
            char* dst = ws + VF_OFF + (size_t)32 * VF_HEAD + ((row0 >> 4) + 4 * wm + 2 * wn + i) * VB;
            if (d < (xo ? 20 : 12)) *(u32x2*)(dst + (g & 1) * 336 + (g >> 1) * 8 + d * 16) = pack4(acc2[i]);
            if (xo == 0 && m == 15) *(u32x2*)(dst + (g & 1) * 336 + (g >> 1) * 8 + 320) = u32x2{0x3F803F80u, 0x3F803F80u};
            if (xo == 0 && lane == 14) *(u32x4*)(dst + 672) = u32x4{0u, 0u, 0u, 0u};
        }
    }
    BARRIER();
    STAMP(1, 4);
}

template <int CH> DI void k1_copy_out(const char* smem, char* dst, unsigned stride, int wid, int lane) {
    lds_cptr src = (lds_cptr)smem + wid * CH + lane * 16;
    char* d = dst + (size_t)wid * stride + lane * 16;
    bf16x8 v[5];
#pragma unroll
    for (int i = 0; i < 5; ++i) v[i] = lds_b128(src + i * 1024);
#pragma unroll
    for (int i = 0; i < 5; ++i) st128<POL1>(d + i * 1024, v[i]);
    if (CH > 5120 && lane < (CH - 5120) / 16) st128<POL1>(d + 5120, lds_b128(src + 5120));
}

extern "C" __global__ __launch_bounds__(512, 2) void proj_kernel(const char* xA, const char* wqkv, const char* xB, const char* wq, const char* wk, const char* wv, char* ws) {
    extern __shared__ __attribute__((aligned(16))) char smem[];
    const int tid = threadIdx.x, lane = tid & 63, wid = (int)rfl(tid >> 6);
    {
        const char* code; CODE_ADDR(code, "proj_kernel");
        if (wid < 3) touch_lines(code + wid * 8192, (wid == 2 ? (lane < 24 ? lane : 23) : lane) * 128, rfl((unsigned)(uintptr_t)smem + 3 * K1_SLOT));
        __builtin_amdgcn_sched_barrier(0);
    }
#define MAP1 0
    const int xcd = blockIdx.x & 7, idx = blockIdx.x >> 3, blk = xcd & 3, hh = xcd >> 2, grp = idx >> 3;
    const bool isB = grp == 1;
    const int rt = 4 * blk + (idx & 3);
    const int ct = (grp < 2 ? 0 : 4 * (grp - 1)) + 2 * hh + ((idx >> 2) & 1);
    STAMP_DECL;
    const int kind = isB ? 0 : ct >> 2, head0 = 8 * (ct & 3), row0 = rt * 128;
    char* dst = kind == 2 ? ws + VF_OFF + (size_t)head0 * VF_HEAD + (row0 >> 4) * VB : kind == 1 ? ws + KF_OFF + (size_t)head0 * KF_HEAD + (row0 >> 5) * KT : ws + QF_OFF + (size_t)((isB ? 32 : 0) + head0) * HEAD40 + row0 * 40;
    const unsigned dstride = kind == 2 ? VF_HEAD : kind == 1 ? KF_HEAD : HEAD40;
    if (wid >= 4) {
        const char* xg = (isB ? xB : xA) + (size_t)row0 * (DM * 2);
        const int wpitch = isB ? DM * 2 : 3 * DM * 2;
        const char* wg = (isB ? wq : wqkv) + ((ct * 320) & ~127);
        const char* w2g = ct >= 2 ? wv : wk;
        const unsigned lds0 = (unsigned)(uintptr_t)smem;
        const char* tdst = (kind == 0 || (row0 & 128) == 0) ? (const char*)((uintptr_t)dst & ~(uintptr_t)127) : nullptr;
        if (isB) k1_loader<11>(xg, wg, w2g, wpitch, wid - 4, lane, lds0, tdst, dstride); else k1_loader<10>(xg, wg, w2g, wpitch, wid - 4, lane, lds0, tdst, dstride);
        asm volatile("s_barrier" ::: "memory");
        { lds_ptr st = (lds_ptr)smem; const int t = tid - 256;
          if (kind == 1 && t < 32) *(__attribute__((address_space(3))) u32x4*)(st + t * KT + 1280) = u32x4{0u, 0u, 0u, 0u};
          if (kind == 2) {
              if (t < 64) *(__attribute__((address_space(3))) u32x4*)(st + t * VB + 672) = u32x4{0u, 0u, 0u, 0u};
              if (t >= 64 && t < 192) *(__attribute__((address_space(3))) u32x4*)(st + ((t - 64) >> 1) * VB + (t & 1) * 336 + 320) = u32x4{0x3F803F80u, 0x3F803F80u, 0x3F803F80u, 0x3F803F80u};
          } }
        BARRIER();
    } else {
        {
            const int bb = blockIdx.x, j0 = lane & 31, j = j0 < 27 ? j0 : 26, s = 2 + 2 * wid + (lane >> 5);
            const char* p;
            if (j < 16) p = (j < 8 ? xA : xB) + (size_t)(8 * bb + (j & 7)) * (DM * 2) + s * 128;
            else if (j < 24) { int n = 8 * bb + j - 16; n = n < 1920 ? n : 1919; const int r = n / 30; p = wqkv + (size_t)(64 * s + r) * (3 * DM * 2) + (n - 30 * r) * 128; }
            else { int n = 3 * bb + j - 24; n = n < 640 ? n : 639; const int r = n / 10; p = wq + (size_t)(64 * s + r) * (DM * 2) + (n - 10 * r) * 128; }
            touch_addr(p, rfl((unsigned)(uintptr_t)smem + 3 * K1_SLOT));
        }
        const int shift32 = (ct & 1) * 2, xo = (ct & 1) * 12;
        if (isB) { if (ct < 2) k1_consumer<0, 1>(ws, smem, wid, lane, shift32, xo, row0, dst, dstride STP_ARG); else k1_consumer<0, 2>(ws, smem, wid, lane, shift32, xo, row0, dst, dstride STP_ARG); }
        else if (kind == 2) k1_consumer<2, 0>(ws, smem, wid, lane, shift32, 0, row0, dst, dstride STP_ARG);
        else if (kind == 1) k1_consumer<1, 0>(ws, smem, wid, lane, shift32, 0, row0, dst, dstride STP_ARG);
        else k1_consumer<0, 0>(ws, smem, wid, lane, shift32, 0, row0, dst, dstride STP_ARG);
    }
    if (kind == 2) k1_copy_out<8 * VB>(smem, dst, dstride, wid, lane);
    else if (kind == 1) k1_copy_out<4 * KT>(smem, dst, dstride, wid, lane);
    else k1_copy_out<5120>(smem, dst, dstride, wid, lane);
    if (wid < 4) { STAMP(1, 5); DIAG_DRAIN(); STAMP(1, 7); STAMP_DUMP(1); }
}

extern "C" void kernel_launch(void* const* d_in, const int* in_sizes, int n_in, void* d_out, int out_size, void* d_ws, size_t ws_size, hipStream_t stream) {
    (void)in_sizes; (void)n_in; (void)out_size; (void)ws_size;
    const char* xA = (const char*)d_in[0]; const char* wqkv = (const char*)d_in[1]; const char* woA = (const char*)d_in[2];
    const char* xB = (const char*)d_in[3]; const char* wq = (const char*)d_in[4]; const char* wk = (const char*)d_in[5]; const char* wv = (const char*)d_in[6]; const char* woB = (const char*)d_in[7];
    char* ws = (char*)d_ws;
    (void)hipFuncSetAttribute((const void*)proj_kernel, hipFuncAttributeMaxDynamicSharedMemorySize, K1_LDS);
    (void)hipFuncSetAttribute((const void*)attn_kernel, hipFuncAttributeMaxDynamicSharedMemorySize, A_LDS);
    (void)hipFuncSetAttribute((const void*)outproj_kernel, hipFuncAttributeMaxDynamicSharedMemorySize, K3_LDS + 256);
    proj_kernel<<<256, 512, K1_LDS, stream>>>(xA, wqkv, xB, wq, wk, wv, ws);
    attn_kernel<<<256, 1024, A_LDS, stream>>>(ws, woA, woB);
    outproj_kernel<<<240, 512, K3_LDS + 256, stream>>>(ws, woA, woB, (char*)d_out);
}
```

```cpp
#include <hip/hip_runtime.h>
#include <stdint.h>

typedef short bf16x8 __attribute__((ext_vector_type(8)));
typedef short bf16x4 __attribute__((ext_vector_type(4)));
typedef float f32x2 __attribute__((ext_vector_type(2)));
typedef float f32x4 __attribute__((ext_vector_type(4)));
typedef float f32x16 __attribute__((ext_vector_type(16)));
typedef unsigned u32x2 __attribute__((ext_vector_type(2)));
typedef unsigned u32x4 __attribute__((ext_vector_type(4)));
typedef __bf16 bf16x2_t __attribute__((ext_vector_type(2)));
typedef short v4i16_t __attribute__((ext_vector_type(4)));
typedef __attribute__((address_space(3))) const char* lds_cptr;
typedef __attribute__((address_space(3))) char* lds_ptr;

#define DI __device__ __forceinline__
#define MAP2 2

constexpr int SEQ = 2048, DM = 640;
constexpr float QSCALE = 0.22360679774997896f * 1.4426950408889634f;

constexpr unsigned HEAD40 = SEQ * 40, KT = 1296, VB = 688, KF_HEAD = 64 * KT, VF_HEAD = 128 * VB;
constexpr unsigned QF_OFF = 0, KF_OFF = QF_OFF + 64 * HEAD40, VF_OFF = KF_OFF + 33 * KF_HEAD, OF_OFF = VF_OFF + 33 * VF_HEAD + 2048;

DI unsigned cvtpk(float lo, float hi) { f32x2 v = {lo, hi}; bf16x2_t b = __builtin_convertvector(v, bf16x2_t); return __builtin_bit_cast(unsigned, b); }
DI u32x2 pack4(f32x4 a) { return u32x2{cvtpk(a[0], a[1]), cvtpk(a[2], a[3])}; }
DI unsigned rfl(unsigned x) { return __builtin_amdgcn_readfirstlane(x); }
DI void glds16(const char* sbase, unsigned voff, unsigned lds_dst) {
    asm volatile("s_mov_b32 m0, %2\n\ts_nop 0\n\tglobal_load_lds_dwordx4 %0, %1" :: "v"(voff), "s"(sbase), "s"(lds_dst) : "memory");
}
template <int N> DI void wait_vm() { asm volatile("s_waitcnt vmcnt(%0)" :: "n"(N) : "memory"); }
DI bf16x8 lds_b128(lds_cptr p) { return *(const __attribute__((address_space(3))) bf16x8*)p; }
DI bf16x4 lds_b64(lds_cptr p) { return *(const volatile __attribute__((address_space(3))) bf16x4*)p; }
DI bf16x4 lds_tr(lds_cptr p) { return __builtin_bit_cast(bf16x4, __builtin_amdgcn_ds_read_tr16_b64_v4i16((__attribute__((address_space(3))) v4i16_t*)p)); }
DI void lds_st64(lds_ptr p, u32x2 v) { *(__attribute__((address_space(3))) u32x2*)p = v; }
DI bf16x8 cat8(bf16x4 a, bf16x4 b) { return __builtin_shufflevector(a, b, 0, 1, 2, 3, 4, 5, 6, 7); }
DI int swap23(int r) { return (r & ~12) | ((r & 4) << 1) | ((r & 8) >> 1); }
DI void touch_lines(const char* p, unsigned voff, unsigned lds_scratch) {
    asm volatile("s_mov_b32 m0, %2\n\ts_nop 0\n\tglobal_load_lds_dword %0, %1" :: "v"(voff), "s"(p), "s"(lds_scratch) : "memory");
}
DI void touch_addr(const char* p, unsigned lds_scratch) {
    asm volatile("s_mov_b32 m0, %1\n\ts_nop 0\n\tglobal_load_lds_dword %0, off" :: "v"(p), "s"(lds_scratch) : "memory");
}
#define CODE_ADDR(var, sym) asm volatile("s_getpc_b64 s[60:61]\n\ts_add_u32 s60, s60, " sym "@rel32@lo+4\n\ts_addc_u32 s61, s61, " sym "@rel32@hi+12\n\ts_mov_b64 %0, s[60:61]" : "=s"(var) :: "scc", "s60", "s61")
template <int POL> DI void st128(char* p, bf16x8 v) {
    if (POL == 0) *(bf16x8*)p = v;
    else if (POL == 1) asm volatile("global_store_dwordx4 %0, %1, off nt\n\ts_nop 1" :: "v"(p), "v"(v) : "memory");
    else if (POL == 2) asm volatile("global_store_dwordx4 %0, %1, off sc1\n\ts_nop 1" :: "v"(p), "v"(v) : "memory");
    else asm volatile("global_store_dwordx4 %0, %1, off sc0 sc1\n\ts_nop 1" :: "v"(p), "v"(v) : "memory");
}
#define POL1 2
#define POL2 0
#define POL3 2
#define MFMA16(a, b, c) __builtin_amdgcn_mfma_f32_16x16x32_bf16(a, b, c, 0, 0, 0)
#define BARRIER() do { asm volatile("" ::: "memory"); __builtin_amdgcn_s_waitcnt(0xC07F); __builtin_amdgcn_s_barrier(); asm volatile("" ::: "memory"); } while (0)
#define SBAR() __builtin_amdgcn_sched_barrier(0)
#define K1_RPM 1
template <int NM, int NR> DI void interleave() {
#pragma unroll
    for (int i = 0; i < NR / K1_RPM; ++i) { __builtin_amdgcn_sched_group_barrier(0x008, 1, 0); __builtin_amdgcn_sched_group_barrier(0x100, K1_RPM, 0); }
    __builtin_amdgcn_sched_group_barrier(0x008, NM - NR / K1_RPM, 0);
    SBAR();
}
#define STAMP_DECL
#define STAMP(kern, slot)
#define STAMP_DUMP(kern)
#define DIAG_DRAIN()

constexpr int A_PART = 6144, A_SLOT = 4 * A_PART, A_X = 3 * A_SLOT, A_LDS = A_X + 16 * 3072;
static_assert(4 * KT <= A_PART && 8 * VB <= A_PART && A_SLOT == 24576, "stage layout");

#define MFMA32(a, b, c) __builtin_amdgcn_mfma_f32_32x32x16_bf16(a, b, c, 0, 0, 0)
#define MFMA32R(a, b, c) __builtin_amdgcn_mfma_f32_32x32x8bf16_1k(a, b, c, 0, 0, 0)

DI void attn_exact(const char* kf, const char* vf, bf16x8 qm, bf16x4 qr, int lane, f32x16& o) {
    const int r32 = lane & 31, h = lane >> 5;
    float mrun = -3.0e38f;
#pragma unroll
    for (int r = 0; r < 16; ++r) o[r] = 0.f;
#pragma unroll 1
    for (int t = 0; t < 64; ++t) {
        const bf16x8 km = *(const bf16x8*)(kf + t * KT + lane * 16);
        const bf16x4 kr = *(const bf16x4*)(kf + t * KT + 1024 + r32 * 8);
        const int vo = r32 < 21 ? h * 336 + r32 * 16 : 672;
        const bf16x8 v0 = *(const bf16x8*)(vf + (2 * t) * VB + vo);
        const bf16x8 v1 = *(const bf16x8*)(vf + (2 * t + 1) * VB + vo);
        f32x16 s;
#pragma unroll
        for (int r = 0; r < 16; ++r) s[r] = 0.f;
        s = MFMA32(km, qm, s);
        s = MFMA32R(kr, qr, s);
        float mx = s[0];
#pragma unroll
        for (int r = 1; r < 16; ++r) mx = fmaxf(mx, s[r]);
        { auto rr = __builtin_amdgcn_permlane32_swap(__float_as_uint(mx), __float_as_uint(mx), false, false); mx = fmaxf(__uint_as_float(rr[0]), __uint_as_float(rr[1])); }
        const float mnew = fmaxf(mrun, mx), alpha = __builtin_amdgcn_exp2f(mrun - mnew);
        mrun = mnew;
        u32x4 p0, p1;
#pragma unroll
        for (int i = 0; i < 4; ++i) {
            p0[i] = cvtpk(__builtin_amdgcn_exp2f(s[2 * i] - mnew), __builtin_amdgcn_exp2f(s[2 * i + 1] - mnew));
            p1[i] = cvtpk(__builtin_amdgcn_exp2f(s[8 + 2 * i] - mnew), __builtin_amdgcn_exp2f(s[8 + 2 * i + 1] - mnew));
        }
#pragma unroll
        for (int r = 0; r < 16; ++r) o[r] *= alpha;
        o = MFMA32(v0, __builtin_bit_cast(bf16x8, p0), o);
        o = MFMA32(v1, __builtin_bit_cast(bf16x8, p1), o);
    }
}

DI float row_sum_of(const f32x16& o) {
    auto rr = __builtin_amdgcn_permlane32_swap(__float_as_uint(o[8]), __float_as_uint(o[8]), false, false);
    return __uint_as_float(rr[1]);
}

#define R_SA "v[64:79]"
#define R_SB "v[80:95]"
#define R_PL "v[100:103]"
#define R_PH "v[104:107]"
#define R_KM "v[108:111]"
#define R_KR "v[112:113]"
#define R_V0 "v[114:117]"
#define R_V1 "v[118:121]"
#define STR_(x) #x
#define STR(x) STR_(x)
#define MF16(d, a, b, c) "v_mfma_f32_32x32x16_bf16 " d ", " a ", " b ", " c "\n\t"
#define MF8(d, a, b, c) "v_mfma_f32_32x32x8_bf16 " d ", " a ", " b ", " c "\n\t"
#define CVT2(p0, p1) "v_cvt_pk_bf16_f32 v" STR(p0) ", v96, v97\n\tv_cvt_pk_bf16_f32 v" STR(p1) ", v98, v99\n\t"
#define EXP4(s0, s1, s2, s3) "v_exp_f32 v96, v" STR(s0) "\n\tv_exp_f32 v97, v" STR(s1) "\n\tv_exp_f32 v98, v" STR(s2) "\n\tv_exp_f32 v99, v" STR(s3) "\n\t"
#define GAP(p0, p1, s0, s1, s2, s3) CVT2(p0, p1) EXP4(s0, s1, s2, s3)
#define WAITL(n) "s_waitcnt lgkmcnt(" STR(n) ")\n\t"
#define RD128(d, a, off) "ds_read_b128 " d ", " a " offset:" STR(off) "\n\t"
#define RD64(d, a, off) "ds_read_b64 " d ", " a " offset:" STR(off) "\n\t"
#define TILE(MID, KOFF, V1OFF, V0OFF) \
    MF16(R_SB, R_KM, "%[qbm]", "0") RD128(R_KM, "v45", KOFF) GAP(106, 107, 64, 65, 66, 67) \
    MF8(R_SB, R_KR, "%[qbr]", R_SB) RD64(R_KR, "v46", KOFF) GAP(100, 101, 68, 69, 70, 71) \
    MF16("%[ob]", R_V1, R_PH, "%[ob]") RD128(R_V1, "v47", V1OFF) GAP(102, 103, 72, 73, 74, 75) \
    WAITL(3) MF16("%[oa]", R_V0, R_PL, "%[oa]") GAP(104, 105, 76, 77, 78, 79) \
    MID \
    WAITL(2) MF16(R_SA, R_KM, "%[qam]", "0") GAP(106, 107, 80, 81, 82, 83) \
    WAITL(1) MF8(R_SA, R_KR, "%[qar]", R_SA) GAP(100, 101, 84, 85, 86, 87) \
    WAITL(0) MF16("%[oa]", R_V1, R_PH, "%[oa]") GAP(102, 103, 88, 89, 90, 91) \
    MF16("%[ob]", R_V0, R_PL, "%[ob]") RD128(R_V0, "v47", V0OFF) GAP(104, 105, 92, 93, 94, 95)
#define TILE_LAST(MID, PRE_K, V1OFF, PRE_V0) \
    MF16(R_SB, R_KM, "%[qbm]", "0") PRE_K RD128(R_KM, "v45", 0) GAP(106, 107, 64, 65, 66, 67) \
    MF8(R_SB, R_KR, "%[qbr]", R_SB) RD64(R_KR, "v46", 0) GAP(100, 101, 68, 69, 70, 71) \
    MF16("%[ob]", R_V1, R_PH, "%[ob]") RD128(R_V1, "v47", V1OFF) GAP(102, 103, 72, 73, 74, 75) \
    WAITL(3) MF16("%[oa]", R_V0, R_PL, "%[oa]") GAP(104, 105, 76, 77, 78, 79) \
    MID \
    WAITL(2) MF16(R_SA, R_KM, "%[qam]", "0") GAP(106, 107, 80, 81, 82, 83) \
    WAITL(1) MF8(R_SA, R_KR, "%[qar]", R_SA) GAP(100, 101, 84, 85, 86, 87) \
    WAITL(0) MF16("%[oa]", R_V1, R_PH, "%[oa]") GAP(102, 103, 88, 89, 90, 91) \
    MF16("%[ob]", R_V0, R_PL, "%[ob]") PRE_V0 RD128(R_V0, "v47", 0) GAP(104, 105, 92, 93, 94, 95)

#define P0 2
#define P1 2
#define P2 1
#define P3 1
#define P4 0
#define P5 0
#define P6 3
#define P7 3
#define PRIO(n) "s_setprio " STR(n) "\n\t"
#define ASM_STAMP(r)
#define TOUCH_PF "global_load_lds_dword v42, %[pf]\n\t"
#define TOUCH_PO "global_load_lds_dword v41, %[po]\n\t"
#define DMA2(SLOTREG) \
    "s_cmp_ge_u32 %[wid], 8\n\ts_cbranch_scc1 5f\n\t" \
    "s_lshr_b32 s46, s45, 1\n\ts_add_u32 s46, s46, %[blk]\n\ts_and_b32 s46, s46, 3\n\ts_lshl_b32 s46, s46, 2\n\t" \
    "s_and_b32 s47, s45, 1\n\ts_add_u32 s46, s46, s47\n\ts_lshl_b32 s46, s46, 2\n\ts_add_u32 s45, s45, 1\n\t" \
    "s_mul_i32 s47, s46, 1296\n\ts_add_u32 s60, s48, s47\n\ts_addc_u32 s61, s49, 0\n\t" \
    "s_add_u32 m0, %[d0], " SLOTREG "\n\ts_mul_i32 s47, s46, %[p1]\n\tglobal_load_lds_dwordx4 v40, s[60:61]\n\t" \
    "s_add_u32 s60, s50, s47\n\ts_addc_u32 s61, s51, 0\n\t" \
    "s_add_u32 m0, m0, 0x2000\n\ts_mul_i32 s47, s46, 1376\n\tglobal_load_lds_dwordx4 v40, s[60:61]\n\t" \
    "s_add_u32 s60, s54, s47\n\ts_addc_u32 s61, s55, 0\n\t" \
    "s_add_u32 m0, m0, 0x2000\n\ts_nop 0\n\tglobal_load_lds_dwordx4 v40, s[60:61]\n\t" \
    "5:\n\t"

extern "C" __global__ __launch_bounds__(1024, 4) void attn_kernel(char* ws, const char* woA, const char* woB) {
    extern __shared__ __attribute__((aligned(16))) char smem[];
    const int tid = threadIdx.x, lane = tid & 63, wid = (int)rfl(tid >> 6);
    const int xcd = blockIdx.x & 7, idx = blockIdx.x >> 3;
    const int qblk = 2 * (xcd & 1) + (idx & 1), branch = idx >> 4, head = 8 * (xcd >> 1) + ((idx >> 1) & 7), kblk = 2 * (xcd & 1);
    const int kvh = branch ? 32 : head;
    const int qg = wid & 7, kh = wid >> 3;
    const unsigned kf_off = KF_OFF + kvh * KF_HEAD, vf_off = VF_OFF + kvh * VF_HEAD;
    const unsigned lds0 = (unsigned)(uintptr_t)smem;
    STAMP_DECL;
    STAMP(2, 0);
    auto psrc = [&](int p) { const int pa = p / 6; return (pa < 2 ? kf_off + pa * 8 * KT : vf_off + (pa - 2) * 16 * VB) + (p - 6 * pa) * 1024; };
    const int w8 = wid & 7;
    const unsigned src0 = rfl(psrc(w8)), src1 = rfl(psrc(w8 + 8)), src2 = rfl(psrc(w8 + 16)), pitch1 = rfl(w8 + 8 < 12 ? KT : 2 * VB);
    const unsigned d0 = rfl(lds0 + wid * 1024);
    const int qrow0 = qblk * 512 + qg * 64;
    const unsigned qoff = rfl(QF_OFF + (unsigned)(branch * 32 + head) * HEAD40 + (unsigned)qrow0 * 40);
    const unsigned kbase = rfl(lds0 + kh * A_PART), vbase = rfl(lds0 + 2 * A_PART + kh * A_PART);
    const char* pf; unsigned pfmax;
    { const char* code; CODE_ADDR(code, "outproj_kernel");
      pf = wid < 4 ? (xcd >> 2 ? woB : woA) + (idx * 200 + wid * 50) * 128 : code + (wid == 5 ? 8192 : 0); pfmax = wid < 4 ? 49 : 63; }
    const char* po = ws + OF_OFF + (size_t)((unsigned)(branch * 32 + head) * HEAD40 + (unsigned)(qrow0 + 32 * kh) * 40);
    bf16x8 qam, qbm; bf16x4 qar, qbr;
    f32x16 oa, ob;
    asm volatile(
        ASM_STAMP("s[72:73]")
        "v_lshlrev_b32 v40, 4, %[lane]\n\t"
        "s_mov_b64 s[52:53], %[ws]\n\t"
        "s_add_u32 s48, s52, %[src0]\n\ts_addc_u32 s49, s53, 0\n\ts_add_u32 s50, s52, %[src1]\n\ts_addc_u32 s51, s53, 0\n\ts_add_u32 s54, s52, %[src2]\n\ts_addc_u32 s55, s53, 0\n\t"
        "s_mov_b32 s44, 0\n\ts_mov_b32 s45, 0\n\t"
        DMA2("s44") ASM_STAMP("s[64:65]")
        "v_and_b32 v41, 31, %[lane]\n\tv_lshrrev_b32 v42, 5, %[lane]\n\t"
        "s_add_u32 s62, s52, %[qoff]\n\ts_addc_u32 s63, s53, 0\n\t"
        "v_lshlrev_b32 v43, 3, v41\n\t"
        "v_mov_b64 %[qar], 0\n\tv_mov_b64 %[qbr], 0\n\t"
        "global_load_dwordx4 %[qam], v40, s[62:63]\n\tglobal_load_dwordx4 %[qbm], v40, s[62:63] offset:1280\n\t"
        "s_mov_b32 exec_hi, 0\n\t"
        "global_load_dwordx2 %[qar], v43, s[62:63] offset:1024\n\tglobal_load_dwordx2 %[qbr], v43, s[62:63] offset:2304\n\t"
        "s_mov_b32 exec_hi, -1\n\t"
        "v_cmp_eq_u32 vcc, 0, v42\n\t"
        "v_add_u32 v45, %[kbase], v40\n\t"
        "v_add_u32 v46, 0x400, v43\n\tv_mov_b32 v48, 0x500\n\ts_nop 0\n\t"
        "v_cndmask_b32 v46, v48, v46, vcc\n\tv_add_u32 v46, %[kbase], v46\n\t"
        "v_cmp_gt_u32 vcc, 21, v41\n\t"
        "v_mul_u32_u24 v47, 0x150, v42\n\tv_lshl_add_u32 v47, v41, 4, v47\n\tv_mov_b32 v48, 0x2a0\n\t"
        "v_cndmask_b32 v47, v48, v47, vcc\n\tv_add_u32 v47, %[vbase], v47\n\t"
        "v_mov_b32 v118, 0\n\tv_mov_b32 v119, 0\n\tv_mov_b32 v120, 0\n\tv_mov_b32 v121, 0\n\t"
        "v_mov_b32 v96, 0\n\tv_mov_b32 v97, 0\n\tv_mov_b32 v98, 0\n\tv_mov_b32 v99, 0\n\t"
        "v_mov_b32 v104, 0\n\tv_mov_b32 v105, 0\n\t"
        "v_mfma_f32_32x32x16_bf16 %[oa], v[118:121], v[118:121], 0\n\tv_mfma_f32_32x32x16_bf16 %[ob], v[118:121], v[118:121], 0\n\t"
        "s_mov_b32 s40, 0\n\t"
        "s_mov_b32 s41, 0\n\t"
        "s_mov_b32 s44, 24576\n\t"
        "s_waitcnt vmcnt(4)\n\t" ASM_STAMP("s[66:67]") "s_barrier\n\t" ASM_STAMP("s[68:69]")
        RD128(R_KM, "v45", 0) RD64(R_KR, "v46", 0) RD128(R_V0, "v47", 0)
        "s_waitcnt vmcnt(0)\n\t"
        DMA2("s44")
        WAITL(0)
        MF16(R_SA, R_KM, "%[qam]", "0") MF8(R_SA, R_KR, "%[qar]", R_SA)
        "s_nop 15\n\ts_nop 3\n\t"
        ASM_STAMP("s[70:71]")
        "2:\n\t"
        "s_add_u32 s42, s41, 24576\n\ts_cmp_eq_u32 s42, 73728\n\ts_cselect_b32 s42, 0, s42\n\t"
        "s_sub_u32 s43, s42, s41\n\t"
        "s_add_u32 s44, s42, 24576\n\ts_cmp_eq_u32 s44, 73728\n\ts_cselect_b32 s44, 0, s44\n\t"
        PRIO(P0) TILE(PRIO(P1), 1296, 688, 1376)
        PRIO(P2) TILE(PRIO(P3), 2592, 2064, 2752)
        PRIO(P4) TILE(PRIO(P5), 3888, 3440, 4128)
        PRIO(P6) TILE_LAST(PRIO(P7),
            "s_waitcnt vmcnt(0)\n\ts_barrier\n\t"
            "v_add_u32 v45, s43, v45\n\tv_add_u32 v46, s43, v46\n\t"
            "s_cmp_ge_u32 s40, 6\n\ts_cbranch_scc1 3f\n\t"
            DMA2("s44")
            "s_branch 4f\n\t"
            "3:\n\t"
            "s_cmp_lg_u32 s40, 6\n\ts_cbranch_scc1 4f\n\t"
            "v_lshrrev_b32 v41, 4, v40\n\tv_min_u32 v42, %[pfmax], v41\n\tv_min_u32 v41, 9, v41\n\tv_lshlrev_b32 v42, 7, v42\n\tv_lshlrev_b32 v41, 7, v41\n\t"
            "s_add_u32 m0, %[d0], s44\n\ts_nop 0\n\t"
            TOUCH_PF TOUCH_PO
            "4:\n\t",
            4816, "v_add_u32 v47, s43, v47\n\t")
        "s_mov_b32 s41, s42\n\t"
        "s_add_u32 s40, s40, 1\n\t"
        "s_cmp_lt_u32 s40, 8\n\ts_cbranch_scc1 2b\n\t"
        "s_nop 1\n\t" CVT2(106, 107) "s_nop 1\n\t"
        MF16("%[ob]", R_V1, R_PH, "%[ob]")
        "s_waitcnt lgkmcnt(0)\n\ts_nop 15\n\ts_nop 3\n\t"
        : [oa] "=&v"(oa), [ob] "=&v"(ob), [qam] "=&v"(qam), [qbm] "=&v"(qbm), [qar] "=&v"(qar), [qbr] "=&v"(qbr)
        : [lane] "v"(lane), [ws] "s"(ws), [src0] "s"(src0), [src1] "s"(src1), [src2] "s"(src2), [p1] "s"(pitch1), [blk] "s"(kblk), [qoff] "s"(qoff),
          [kbase] "s"(kbase), [vbase] "s"(vbase), [d0] "s"(d0), [wid] "s"(wid), [pf] "s"(pf), [po] "s"(po), [pfmax] "s"(pfmax)
        : "memory", "scc", "vcc", "s40", "s41", "s42", "s43", "s44", "s45", "s46", "s47", "s48", "s49", "s50", "s51", "s52", "s53", "s54", "s55", "s60", "s61", "s62", "s63",
          "s64", "s65", "s66", "s67", "s68", "s69", "s70", "s71", "s72", "s73",
          "v40", "v41", "v42", "v43", "v44", "v45", "v46", "v47", "v48",
          "v64", "v65", "v66", "v67", "v68", "v69", "v70", "v71", "v72", "v73", "v74", "v75", "v76", "v77", "v78", "v79",
          "v80", "v81", "v82", "v83", "v84", "v85", "v86", "v87", "v88", "v89", "v90", "v91", "v92", "v93", "v94", "v95",
          "v96", "v97", "v98", "v99", "v100", "v101", "v102", "v103", "v104", "v105", "v106", "v107", "v108", "v109", "v110", "v111",
          "v112", "v113", "v114", "v115", "v116", "v117", "v118", "v119", "v120", "v121");
    STAMP(2, 2);
    const int r32 = lane & 31, h = lane >> 5;
    f32x16 o;
    {
        lds_ptr xb = (lds_ptr)smem + A_X + wid * 3072 + lane * 16;
        if (kh) {
#pragma unroll
            for (int i = 0; i < 3; ++i) *(__attribute__((address_space(3))) f32x4*)(xb + i * 1024) = f32x4{oa[4 * i], oa[4 * i + 1], oa[4 * i + 2], oa[4 * i + 3]};
            o = ob;
        } else {
#pragma unroll
            for (int i = 0; i < 3; ++i) *(__attribute__((address_space(3))) f32x4*)(xb + i * 1024) = f32x4{ob[4 * i], ob[4 * i + 1], ob[4 * i + 2], ob[4 * i + 3]};
            o = oa;
        }
    }
    BARRIER();
    {
        lds_cptr xb = (lds_cptr)smem + A_X + (wid ^ 8) * 3072 + lane * 16;
#pragma unroll
        for (int i = 0; i < 3; ++i) { const f32x4 v = *(const __attribute__((address_space(3))) f32x4*)(xb + i * 1024);
#pragma unroll
            for (int e = 0; e < 4; ++e) o[4 * i + e] += v[e]; }
    }
    float l = row_sum_of(o);
    float mag = 0.f;
#pragma unroll
    for (int r = 0; r < 12; ++r) mag += fabsf(o[r]);
    const bool ok = l >= 0x1p-60f && l <= 0x1p60f && mag <= 3.0e38f;
    if (__builtin_expect(!__all(ok), 0)) {
        attn_exact(ws + kf_off, ws + vf_off, kh ? qbm : qam, kh ? qbr : qar, lane, o);
        l = row_sum_of(o);
    }
    const float inv = __builtin_amdgcn_rcpf(l);
    {
        lds_ptr so = (lds_ptr)smem + A_X + (wid ^ 8) * 3072;
        lds_st64(so + r32 * 40 + 8 * h, u32x2{cvtpk(o[0] * inv, o[1] * inv), cvtpk(o[2] * inv, o[3] * inv)});
        lds_st64(so + r32 * 40 + 16 + 8 * h, u32x2{cvtpk(o[4] * inv, o[5] * inv), cvtpk(o[6] * inv, o[7] * inv)});
        if (h == 0) lds_st64(so + r32 * 40 + 32, u32x2{cvtpk(o[8] * inv, o[9] * inv), cvtpk(o[10] * inv, o[11] * inv)});
        asm volatile("" ::: "memory"); __builtin_amdgcn_s_waitcnt(0xC07F); asm volatile("" ::: "memory");
        char* od = ws + OF_OFF + (size_t)((unsigned)(branch * 32 + head) * HEAD40 + (unsigned)(qrow0 + 32 * kh) * 40) + lane * 16;
        st128<POL2>(od, lds_b128((lds_cptr)so + lane * 16));
        if (lane < 16) st128<POL2>(od + 1024, lds_b128((lds_cptr)so + 1024 + lane * 16));
    }
    DIAG_DRAIN();
    STAMP(2, 7);
    STAMP_DUMP(2);
}

#define K3_RPM 2
constexpr int K3_X = 0, K3_W = 32 * 3840, K3_LDS = K3_W + 4 * 8192;
constexpr int k3_total(int t) { int n = 14; for (int u = 0; u < t; ++u) n += (u <= 16 ? 2 : 0) + (u <= 10 ? 2 : 0); return n; }
constexpr int k3_allowed(int t) {
    int last = t <= 2 ? 10 + 2 * t : k3_total(t - 3) + 2;
    const int xg = t == 5 ? 3 : t == 10 ? 7 : t == 15 ? 10 : -1;
    if (xg >= 0) { const int e = k3_total(xg) + 4; if (e > last) last = e; }
    return k3_total(t) - last;
}
DI int k3_krow(int c, int e) { const int P = c / 5, r = c - 5 * P; return r < 4 ? (2 * P + (r >> 1)) * 20 + (r & 1) * 8 + e : (2 * P + (e >> 2)) * 20 + 16 + (e & 3); }

DI unsigned k3_xoff(int o, int rot) { const int hd = o / 3840; return (unsigned)(((hd + 8 * rot) & 31) * HEAD40 + (o - hd * 3840)); }
template <int T> DI void k3_load_steps(const char* xsrc, const char* wsrc, const unsigned (&wo)[5][2], int lw, int lane, unsigned lds0, int rot) {
    if constexpr (T < 20) {
        wait_vm<k3_allowed(T)>();
        asm volatile("s_barrier" ::: "memory");
        if constexpr (T + 3 < 20) {
            constexpr int t = T + 3;
#pragma unroll
            for (int i = 0; i < 2; ++i) glds16(wsrc + (size_t)((t / 5 + rot) & 3) * 160 * (DM * 2), wo[t % 5][i], lds0 + K3_W + (t & 3) * 8192 + (2 * lw + i) * 1024);
        }
        if constexpr (T <= 10) {
#pragma unroll
            for (int i = 0; i < 2; ++i) { const int pc = lw + 4 * (8 + 2 * T + i); glds16(xsrc, k3_xoff(pc * 1024 + lane * 16, rot), lds0 + K3_X + pc * 1024); }
        }
        k3_load_steps<T + 1>(xsrc, wsrc, wo, lw, lane, lds0, rot);
    }
}

extern "C" __global__ __launch_bounds__(512, 2) void outproj_kernel(char* ws, const char* woA, const char* woB, char* out) {
    extern __shared__ __attribute__((aligned(16))) char smem[];
    const int tid = threadIdx.x, lane = tid & 63, wid = (int)rfl(tid >> 6);
    const int xcd = blockIdx.x & 7, idx = blockIdx.x >> 3, br = xcd >> 2;
    const int blk = 2 * (xcd & 1) + ((xcd >> 1) & 1), rot = xcd >> 1;
    if (idx >= 30) return;
    STAMP_DECL;
    const int rl = idx / 5, ct = idx - 5 * rl, row0 = blk * 512 + (rl < 5 ? rl * 96 : 416);
    if (wid >= 4) {
        const int lw = wid - 4;
        const char* xsrc = ws + OF_OFF + (size_t)br * 32 * HEAD40 + row0 * 40;
        const char* wsrc = (br ? woB : woA) + ct * 256;
        const unsigned lds0 = (unsigned)(uintptr_t)smem;
        unsigned wo[5][2];
#pragma unroll
        for (int tt = 0; tt < 5; ++tt)
#pragma unroll
            for (int i = 0; i < 2; ++i) {
                const int rho = 4 * (2 * lw + i) + (lane >> 4), s16 = lane & 15, kap = swap23(rho);
                wo[tt][i] = (unsigned)(k3_krow(4 * tt + (kap >> 3), kap & 7) * (DM * 2) + (((s16 >> 1) ^ (rho & 7)) << 5) + (s16 & 1) * 16);
            }
#pragma unroll
        for (int j = 0; j < 8; ++j) { const int pc = lw + 4 * j; glds16(xsrc, k3_xoff(pc * 1024 + lane * 16, rot), lds0 + K3_X + pc * 1024); }
#pragma unroll
        for (int t = 0; t < 3; ++t)
#pragma unroll
            for (int i = 0; i < 2; ++i) glds16(wsrc + (size_t)rot * 160 * (DM * 2), wo[t][i], lds0 + K3_W + t * 8192 + (2 * lw + i) * 1024);
        k3_load_steps<0>(xsrc, wsrc, wo, lw, lane, lds0, rot);
        asm volatile("s_barrier\n\ts_barrier" ::: "memory");
    } else {
        lds_cptr lds = (lds_cptr)smem;
        const int wm = wid >> 1, wn = wid & 1, m = lane & 15, g = lane >> 4, q = (lane & 15) >> 2, p = lane & 3;
        unsigned xlo[5], xhi[5], wa[4];
#pragma unroll
        for (int tt = 0; tt < 5; ++tt) {
            const int c = 4 * tt + g, P = c / 5, r = c - 5 * P, rb = (48 * wm + m) * 40;
            xlo[tt] = (unsigned)(K3_X + rb + (r < 4 ? (2 * P + (r >> 1)) * 3840 + (r & 1) * 16 : 2 * P * 3840 + 32));
            xhi[tt] = (unsigned)(K3_X + rb + (r < 4 ? (2 * P + (r >> 1)) * 3840 + (r & 1) * 16 + 8 : (2 * P + 1) * 3840 + 32));
        }
        { const int sx = (g & 1) * 4 + q;
#pragma unroll
          for (int j = 0; j < 4; ++j) wa[j] = (unsigned)(K3_W + ((g >> 1) * 16 + sx) * 256 + (((4 * wn + j) ^ sx) << 5) + p * 8); }
        f32x4 acc[3][4];
#pragma unroll
        for (int i = 0; i < 3; ++i)
#pragma unroll
            for (int j = 0; j < 4; ++j) acc[i][j] = f32x4{0.f, 0.f, 0.f, 0.f};
        bf16x8 xf[2][3], wf[2][4];
        auto load = [&](int t) {
            const int tt = t % 5, u = t / 5;
#pragma unroll
            for (int i = 0; i < 3; ++i) xf[t & 1][i] = cat8(lds_b64(lds + xlo[tt] + u * 30720 + i * 640), lds_b64(lds + xhi[tt] + u * 30720 + i * 640));
#pragma unroll
            for (int j = 0; j < 4; ++j) wf[t & 1][j] = cat8(lds_tr(lds + wa[j] + (t & 3) * 8192), lds_tr(lds + wa[j] + (t & 3) * 8192 + 2048));
        };
        STAMP(3, 0);
        BARRIER();
        STAMP(3, 1);
        load(0);
        SBAR();
#pragma unroll
        for (int t = 0; t < 20; ++t) {
            if (t + 1 < 20) { BARRIER(); load(t + 1); }
#pragma unroll
            for (int i = 0; i < 3; ++i)
#pragma unroll
                for (int j = 0; j < 4; ++j) acc[i][j] = MFMA16(wf[t & 1][j], xf[t & 1][i], acc[i][j]);
            if (t + 1 < 20) {
#pragma unroll
                for (int i = 0; i < 14 / K3_RPM; ++i) { __builtin_amdgcn_sched_group_barrier(0x100, K3_RPM, 0); __builtin_amdgcn_sched_group_barrier(0x008, 1, 0); }
                __builtin_amdgcn_sched_group_barrier(0x100, 14, 0); __builtin_amdgcn_sched_group_barrier(0x008, 12, 0);
            }
            SBAR();
        }
        STAMP(3, 2);
        BARRIER();
        STAMP(3, 3);
        lds_ptr st = (lds_ptr)smem;
#pragma unroll
        for (int i = 0; i < 3; ++i) {
            const int row = 48 * wm + 16 * i + m;
#pragma unroll
            for (int j = 0; j < 4; ++j) { const int b = (64 * wn + 16 * j + 4 * g) * 2; lds_st64(st + row * 256 + ((((b >> 4) ^ (row & 15)) << 4) | (b & 8)), pack4(acc[i][j])); }
        }
        BARRIER();
        STAMP(3, 4);
    }
#pragma unroll
    for (int it = 0; it < 3; ++it) {
        const int o = it * 8192 + tid * 16, row = o >> 8, s = (o >> 4) & 15;
        st128<POL3>(out + (size_t)(row0 + row) * 2560 + br * 1280 + ct * 256 + ((s ^ (row & 15)) << 4), lds_b128((lds_cptr)smem + o));
    }
    if (wid < 4) { STAMP(3, 5); DIAG_DRAIN(); STAMP(3, 7); STAMP_DUMP(3); }
}

constexpr int NSTAGE = DM / 64;
constexpr int K1_X = 0, K1_W = 16384, K1_W2 = K1_W + 24576, K1_SLOT = K1_W2 + 3072, K1_LDS = 3 * K1_SLOT + 256;

template <int NP> DI void k1_loader(const char* xg, const char* wg, const char* w2g, int wpitch, int lw, int lane, unsigned lds0, const char* tdst, unsigned dstride) {
    unsigned xo[4], wo[6], w2o = 0;
#pragma unroll
    for (int i = 0; i < 4; ++i) { const int row = (4 * lw + i) * 8 + (lane >> 3), pos = lane & 7; xo[i] = (unsigned)(row * (DM * 2) + ((pos ^ ((row >> 1) & 7)) << 4)); }
#pragma unroll
    for (int i = 0; i < 6; ++i) {
        const int o = (6 * lw + i) * 1024 + lane * 16, rho = o / 384, b = o - rho * 384;
        wo[i] = (unsigned)(swap23(rho) * wpitch + (((b >> 5) ^ ((rho >> 1) & 3)) << 5) + (b & 16));
    }
    const int p2 = lw < 3 ? lw : 2;
    if (NP == 11) { const int o = p2 * 1024 + lane * 16; w2o = (unsigned)(o < 2560 ? o : 2544); }
    auto issue = [&](int s, int slot) {
        const unsigned sl = lds0 + (unsigned)slot * K1_SLOT;
        const char* xs = xg + (size_t)s * 128; const char* wsrc = wg + (size_t)s * 64 * wpitch;
#pragma unroll
        for (int i = 0; i < 4; ++i) glds16(xs, xo[i], sl + K1_X + (4 * lw + i) * 1024);
#pragma unroll
        for (int i = 0; i < 6; ++i) glds16(wsrc, wo[i], sl + K1_W + (6 * lw + i) * 1024);
        if (NP == 11) glds16(w2g + (size_t)s * 2560, w2o, sl + K1_W2 + p2 * 1024);
    };
    issue(0, 0); issue(1, 1);
    int nslot = 2;
#pragma unroll 1
    for (int s = 0; s < NSTAGE; ++s) {
        if (s < NSTAGE - 1) wait_vm<NP>(); else wait_vm<0>();
        asm volatile("s_barrier" ::: "memory");
        if (s + 2 < NSTAGE) { issue(s + 2, nslot); nslot = nslot == 2 ? 0 : nslot + 1; }
    }
}

#define TOUCH_ALL 0
struct K1Frag { bf16x8 x[4]; bf16x8 w[5]; bf16x8 w2; };

#define STP_PARAM
#define STP_ARG
template <int KIND, int XTRA> DI void k1_consumer(char* ws, char* smem, int wid, int lane, int shift32, int xo, int row0, const char* dst, unsigned dstride STP_PARAM) {
    lds_cptr lds = (lds_cptr)smem;
    const int wm = wid >> 1, wn = wid & 1, m = lane & 15, g = lane >> 4, q = (lane & 15) >> 2, p = lane & 3;
    const unsigned xa0 = K1_X + (unsigned)((64 * wm + m) * 128 + ((g ^ ((m >> 1) & 7)) << 4));
    unsigned wa[5];
    { const int gx = ((g & 1) << 1) | (q >> 1);
#pragma unroll
      for (int j = 0; j < 5; ++j) wa[j] = K1_W + (unsigned)(((g >> 1) * 16 + (g & 1) * 4 + q) * 384 + (((shift32 + 5 * wn + j) ^ gx) << 5) + p * 8); }
    const unsigned w2a = K1_W2 + (unsigned)((8 * g + q) * 40 + p * 8 + xo * 2);
    f32x4 acc[4][5], acc2[2];
#pragma unroll
    for (int i = 0; i < 4; ++i)
#pragma unroll
        for (int j = 0; j < 5; ++j) acc[i][j] = f32x4{0.f, 0.f, 0.f, 0.f};
    acc2[0] = acc2[1] = f32x4{0.f, 0.f, 0.f, 0.f};
    auto load = [&](K1Frag& f, unsigned sl, int kk) {
        lds_cptr b = lds + sl;
#pragma unroll
        for (int i = 0; i < 4; ++i) f.x[i] = lds_b128(b + ((xa0 ^ (kk * 64)) + i * 2048));
#pragma unroll
        for (int j = 0; j < 5; ++j) f.w[j] = cat8(lds_tr(b + wa[j] + kk * 12288), lds_tr(b + wa[j] + kk * 12288 + 3072));
        if (XTRA) f.w2 = cat8(lds_tr(b + w2a + kk * 1280), lds_tr(b + w2a + kk * 1280 + 160));
    };
    auto mma = [&](const K1Frag& f) {
#pragma unroll
        for (int i = 0; i < 4; ++i)
#pragma unroll
            for (int j = 0; j < 5; ++j) acc[i][j] = KIND == 2 ? MFMA16(f.x[i], f.w[j], acc[i][j]) : MFMA16(f.w[j], f.x[i], acc[i][j]);
        if (XTRA) {
#pragma unroll
            for (int i = 0; i < 2; ++i) { const bf16x8 xx = wn ? f.x[2 + i] : f.x[i]; acc2[i] = XTRA == 2 ? MFMA16(xx, f.w2, acc2[i]) : MFMA16(f.w2, xx, acc2[i]); }
        }
    };
    constexpr int CH = KIND == 0 ? 5120 : KIND == 1 ? 4 * KT : 8 * VB, TP = KIND == 0 ? 1280 : KT;
    unsigned sa[5], sh[5];
#pragma unroll
    for (int j = 0; j < 5; ++j) {
        const int col = 80 * wn + 16 * j + (KIND == 2 ? m : 4 * g), hl = (col * 205) >> 12, d = col - hl * 20;
        if (KIND == 2) { sa[j] = (unsigned)(hl * CH + 4 * wm * VB + (g & 1) * 336 + (g >> 1) * 8 + d * 16); sh[j] = 0; }
        else { const bool rem = d == 16; sa[j] = (unsigned)(hl * CH + 2 * wm * TP + (rem ? 1024 + m * 8 : (d >> 3) * 512 + m * 16 + (d & 4) * 2)); sh[j] = sa[j] + (rem ? 128 : 256); asm volatile("" : "+v"(sh[j])); }
        asm volatile("" : "+v"(sa[j]));
    }
    K1Frag f0, f1;
    STAMP(1, 0);
    BARRIER();
    STAMP(1, 1);
    const unsigned scratch = rfl((unsigned)(uintptr_t)smem + 3 * K1_SLOT);
    load(f0, 0, 0);
    int slot = 0;
    constexpr int NM = XTRA ? 22 : 20, NR = XTRA ? 16 : 14;
    SBAR();
#pragma unroll 1
    for (int s = 0; s < NSTAGE - 1; ++s) {
        load(f1, (unsigned)slot * K1_SLOT, 1);
        mma(f0);
        interleave<NM, NR>();
        slot = slot == 2 ? 0 : slot + 1;
        BARRIER();
        if (wid == (s & 3)) {
            if (s == 8) { const char* code; CODE_ADDR(code, "attn_kernel"); touch_lines(code, lane * 128, scratch); }
            SBAR();
        }
        load(f0, (unsigned)slot * K1_SLOT, 0);
        mma(f1);
        interleave<NM, NR>();
    }
    load(f1, (unsigned)slot * K1_SLOT, 1);
    mma(f0);
    interleave<NM, NR>();
    mma(f1);
    STAMP(1, 2);
    BARRIER();
    STAMP(1, 3);
    lds_ptr st = (lds_ptr)smem;
#pragma unroll
    for (int j = 0; j < 5; ++j) {
        if (KIND == 2) {
#pragma unroll
            for (int i = 0; i < 4; ++i) lds_st64(st + sa[j] + i * VB, pack4(acc[i][j]));
        } else {
            const float sc = KIND == 0 ? QSCALE : 1.0f;
            lds_st64(st + sa[j], pack4(acc[0][j] * sc)); lds_st64(st + sa[j] + TP, pack4(acc[2][j] * sc));
            lds_st64(st + sh[j], pack4(acc[1][j] * sc)); lds_st64(st + sh[j] + TP, pack4(acc[3][j] * sc));
        }
    }
    if (XTRA == 1) {
        const int d0 = xo + 4 * g;
        if (d0 < (xo ? 20 : 12)) {
#pragma unroll
            for (int i = 0; i < 2; ++i) {
                const int row = row0 + 64 * wm + 32 * wn + 16 * i + m, key = row & 31;
                *(u32x2*)(ws + KF_OFF + (size_t)32 * KF_HEAD + (row >> 5) * KT + (d0 < 16 ? (d0 >> 3) * 512 + key * 16 + (d0 & 4) * 2 : 1024 + key * 8)) = pack4(acc2[i]);
            }
        }
        if (xo == 0 && lane == 0 && wn == 0) {
#pragma unroll
            for (int i = 0; i < 2; ++i) *(u32x4*)(ws + KF_OFF + (size_t)32 * KF_HEAD + ((row0 >> 5) + 2 * wm + i) * KT + 1280) = u32x4{0u, 0u, 0u, 0u};
        }
    }
    if (XTRA == 2) {
        const int d = xo + m;
#pragma unroll
        for (int i = 0; i < 2; ++i) {
            char* dst = ws + VF_OFF + (size_t)32 * VF_HEAD + ((row0 >> 4) + 4 * wm + 2 * wn + i) * VB;
            if (d < (xo ? 20 : 12)) *(u32x2*)(dst + (g & 1) * 336 + (g >> 1) * 8 + d * 16) = pack4(acc2[i]);
            if (xo == 0 && m == 15) *(u32x2*)(dst + (g & 1) * 336 + (g >> 1) * 8 + 320) = u32x2{0x3F803F80u, 0x3F803F80u};
            if (xo == 0 && lane == 14) *(u32x4*)(dst + 672) = u32x4{0u, 0u, 0u, 0u};
        }
    }
    BARRIER();
    STAMP(1, 4);
}

template <int CH> DI void k1_copy_out(const char* smem, char* dst, unsigned stride, int wid, int lane) {
    lds_cptr src = (lds_cptr)smem + wid * CH + lane * 16;
    char* d = dst + (size_t)wid * stride + lane * 16;
    bf16x8 v[5];
#pragma unroll
    for (int i = 0; i < 5; ++i) v[i] = lds_b128(src + i * 1024);
#pragma unroll
    for (int i = 0; i < 5; ++i) st128<POL1>(d + i * 1024, v[i]);
    if (CH > 5120 && lane < (CH - 5120) / 16) st128<POL1>(d + 5120, lds_b128(src + 5120));
}

extern "C" __global__ __launch_bounds__(512, 2) void proj_kernel(const char* xA, const char* wqkv, const char* xB, const char* wq, const char* wk, const char* wv, char* ws) {
    extern __shared__ __attribute__((aligned(16))) char smem[];
    const int tid = threadIdx.x, lane = tid & 63, wid = (int)rfl(tid >> 6);
#define MAP1 0
    const int xcd = blockIdx.x & 7, idx = blockIdx.x >> 3, blk = xcd & 3, hh = xcd >> 2, grp = idx >> 3;
    const bool isB = grp == 1;
    const int rt = 4 * blk + (idx & 3);
    const int ct = (grp < 2 ? 0 : 4 * (grp - 1)) + 2 * hh + ((idx >> 2) & 1);
    STAMP_DECL;
    const int kind = isB ? 0 : ct >> 2, head0 = 8 * (ct & 3), row0 = rt * 128;
    char* dst = kind == 2 ? ws + VF_OFF + (size_t)head0 * VF_HEAD + (row0 >> 4) * VB : kind == 1 ? ws + KF_OFF + (size_t)head0 * KF_HEAD + (row0 >> 5) * KT : ws + QF_OFF + (size_t)((isB ? 32 : 0) + head0) * HEAD40 + row0 * 40;
    const unsigned dstride = kind == 2 ? VF_HEAD : kind == 1 ? KF_HEAD : HEAD40;
    if (wid >= 4) {
        const char* xg = (isB ? xB : xA) + (size_t)row0 * (DM * 2);
        const int wpitch = isB ? DM * 2 : 3 * DM * 2;
        const char* wg = (isB ? wq : wqkv) + ((ct * 320) & ~127);
        const char* w2g = ct >= 2 ? wv : wk;
        const unsigned lds0 = (unsigned)(uintptr_t)smem;
        const char* tdst = (kind == 0 || (row0 & 128) == 0) ? (const char*)((uintptr_t)dst & ~(uintptr_t)127) : nullptr;
        if (isB) k1_loader<11>(xg, wg, w2g, wpitch, wid - 4, lane, lds0, tdst, dstride); else k1_loader<10>(xg, wg, w2g, wpitch, wid - 4, lane, lds0, tdst, dstride);
        asm volatile("s_barrier" ::: "memory");
        { lds_ptr st = (lds_ptr)smem; const int t = tid - 256;
          if (kind == 1 && t < 32) *(__attribute__((address_space(3))) u32x4*)(st + t * KT + 1280) = u32x4{0u, 0u, 0u, 0u};
          if (kind == 2) {
              if (t < 64) *(__attribute__((address_space(3))) u32x4*)(st + t * VB + 672) = u32x4{0u, 0u, 0u, 0u};
              if (t >= 64 && t < 192) *(__attribute__((address_space(3))) u32x4*)(st + ((t - 64) >> 1) * VB + (t & 1) * 336 + 320) = u32x4{0x3F803F80u, 0x3F803F80u, 0x3F803F80u, 0x3F803F80u};
          } }
        BARRIER();
    } else {
        {
            const char* code; CODE_ADDR(code, "proj_kernel");
            if (wid < 3) touch_lines(code + wid * 8192, (wid == 2 ? (lane < 24 ? lane : 23) : lane) * 128, rfl((unsigned)(uintptr_t)smem + 3 * K1_SLOT));
        }
        {
            const int bb = blockIdx.x, j0 = lane & 31, j = j0 < 27 ? j0 : 26, s = 2 + 2 * wid + (lane >> 5);
            const char* p;
            if (j < 16) p = (j < 8 ? xA : xB) + (size_t)(8 * bb + (j & 7)) * (DM * 2) + s * 128;
            else if (j < 24) { int n = 8 * bb + j - 16; n = n < 1920 ? n : 1919; const int r = n / 30; p = wqkv + (size_t)(64 * s + r) * (3 * DM * 2) + (n - 30 * r) * 128; }
            else { int n = 3 * bb + j - 24; n = n < 640 ? n : 639; const int r = n / 10; p = wq + (size_t)(64 * s + r) * (DM * 2) + (n - 10 * r) * 128; }
            touch_addr(p, rfl((unsigned)(uintptr_t)smem + 3 * K1_SLOT));
        }
        const int shift32 = (ct & 1) * 2, xo = (ct & 1) * 12;
        if (isB) { if (ct < 2) k1_consumer<0, 1>(ws, smem, wid, lane, shift32, xo, row0, dst, dstride STP_ARG); else k1_consumer<0, 2>(ws, smem, wid, lane, shift32, xo, row0, dst, dstride STP_ARG); }
        else if (kind == 2) k1_consumer<2, 0>(ws, smem, wid, lane, shift32, 0, row0, dst, dstride STP_ARG);
        else if (kind == 1) k1_consumer<1, 0>(ws, smem, wid, lane, shift32, 0, row0, dst, dstride STP_ARG);
        else k1_consumer<0, 0>(ws, smem, wid, lane, shift32, 0, row0, dst, dstride STP_ARG);
    }
    if (kind == 2) k1_copy_out<8 * VB>(smem, dst, dstride, wid, lane);
    else if (kind == 1) k1_copy_out<4 * KT>(smem, dst, dstride, wid, lane);
    else k1_copy_out<5120>(smem, dst, dstride, wid, lane);
    if (wid < 4) { STAMP(1, 5); DIAG_DRAIN(); STAMP(1, 7); STAMP_DUMP(1); }
}

extern "C" void kernel_launch(void* const* d_in, const int* in_sizes, int n_in, void* d_out, int out_size, void* d_ws, size_t ws_size, hipStream_t stream) {
    (void)in_sizes; (void)n_in; (void)out_size; (void)ws_size;
    const char* xA = (const char*)d_in[0]; const char* wqkv = (const char*)d_in[1]; const char* woA = (const char*)d_in[2];
    const char* xB = (const char*)d_in[3]; const char* wq = (const char*)d_in[4]; const char* wk = (const char*)d_in[5]; const char* wv = (const char*)d_in[6]; const char* woB = (const char*)d_in[7];
    char* ws = (char*)d_ws;
    (void)hipFuncSetAttribute((const void*)proj_kernel, hipFuncAttributeMaxDynamicSharedMemorySize, K1_LDS);
    (void)hipFuncSetAttribute((const void*)attn_kernel, hipFuncAttributeMaxDynamicSharedMemorySize, A_LDS);
    (void)hipFuncSetAttribute((const void*)outproj_kernel, hipFuncAttributeMaxDynamicSharedMemorySize, K3_LDS + 256);
    proj_kernel<<<256, 512, K1_LDS, stream>>>(xA, wqkv, xB, wq, wk, wv, ws);
    attn_kernel<<<256, 1024, A_LDS, stream>>>(ws, woA, woB);
    outproj_kernel<<<240, 512, K3_LDS + 256, stream>>>(ws, woA, woB, (char*)d_out);
}
```

```cpp
#include <hip/hip_runtime.h>
#include <stdint.h>

typedef short bf16x8 __attribute__((ext_vector_type(8)));
typedef short bf16x4 __attribute__((ext_vector_type(4)));
typedef float f32x2 __attribute__((ext_vector_type(2)));
typedef float f32x4 __attribute__((ext_vector_type(4)));
typedef float f32x16 __attribute__((ext_vector_type(16)));
typedef unsigned u32x2 __attribute__((ext_vector_type(2)));
typedef unsigned u32x4 __attribute__((ext_vector_type(4)));
typedef __bf16 bf16x2_t __attribute__((ext_vector_type(2)));
typedef short v4i16_t __attribute__((ext_vector_type(4)));
typedef __attribute__((address_space(3))) const char* lds_cptr;
typedef __attribute__((address_space(3))) char* lds_ptr;

#define DI __device__ __forceinline__
#define MAP2 2

constexpr int SEQ = 2048, DM = 640;
constexpr float QSCALE = 0.22360679774997896f * 1.4426950408889634f;

constexpr unsigned HEAD40 = SEQ * 40, KT = 1296, VB = 688, KF_HEAD = 64 * KT, VF_HEAD = 128 * VB;
constexpr unsigned QF_OFF = 0, KF_OFF = QF_OFF + 64 * HEAD40, VF_OFF = KF_OFF + 33 * KF_HEAD, OF_OFF = VF_OFF + 33 * VF_HEAD + 2048;

DI unsigned cvtpk(float lo, float hi) { f32x2 v = {lo, hi}; bf16x2_t b = __builtin_convertvector(v, bf16x2_t); return __builtin_bit_cast(unsigned, b); }
DI u32x2 pack4(f32x4 a) { return u32x2{cvtpk(a[0], a[1]), cvtpk(a[2], a[3])}; }
DI unsigned rfl(unsigned x) { return __builtin_amdgcn_readfirstlane(x); }
DI void glds16(const char* sbase, unsigned voff, unsigned lds_dst) {
    asm volatile("s_mov_b32 m0, %2\n\ts_nop 0\n\tglobal_load_lds_dwordx4 %0, %1" :: "v"(voff), "s"(sbase), "s"(lds_dst) : "memory");
}
template <int N> DI void wait_vm() { asm volatile("s_waitcnt vmcnt(%0)" :: "n"(N) : "memory"); }
DI bf16x8 lds_b128(lds_cptr p) { return *(const __attribute__((address_space(3))) bf16x8*)p; }
DI bf16x4 lds_b64(lds_cptr p) { return *(const volatile __attribute__((address_space(3))) bf16x4*)p; }
DI bf16x4 lds_tr(lds_cptr p) { return __builtin_bit_cast(bf16x4, __builtin_amdgcn_ds_read_tr16_b64_v4i16((__attribute__((address_space(3))) v4i16_t*)p)); }
DI void lds_st64(lds_ptr p, u32x2 v) { *(__attribute__((address_space(3))) u32x2*)p = v; }
DI bf16x8 cat8(bf16x4 a, bf16x4 b) { return __builtin_shufflevector(a, b, 0, 1, 2, 3, 4, 5, 6, 7); }
DI int swap23(int r) { return (r & ~12) | ((r & 4) << 1) | ((r & 8) >> 1); }
DI void touch_lines(const char* p, unsigned voff, unsigned lds_scratch) {
    asm volatile("s_mov_b32 m0, %2\n\ts_nop 0\n\tglobal_load_lds_dword %0, %1" :: "v"(voff), "s"(p), "s"(lds_scratch) : "memory");
}
DI void touch_addr(const char* p, unsigned lds_scratch) {
    asm volatile("s_mov_b32 m0, %1\n\ts_nop 0\n\tglobal_load_lds_dword %0, off" :: "v"(p), "s"(lds_scratch) : "memory");
}
#define CODE_ADDR(var, sym) asm volatile("s_getpc_b64 s[60:61]\n\ts_add_u32 s60, s60, " sym "@rel32@lo+4\n\ts_addc_u32 s61, s61, " sym "@rel32@hi+12\n\ts_mov_b64 %0, s[60:61]" : "=s"(var) :: "scc", "s60", "s61")
template <int POL> DI void st128(char* p, bf16x8 v) {
    if (POL == 0) *(bf16x8*)p = v;
    else if (POL == 1) asm volatile("global_store_dwordx4 %0, %1, off nt\n\ts_nop 1" :: "v"(p), "v"(v) : "memory");
    else if (POL == 2) asm volatile("global_store_dwordx4 %0, %1, off sc1\n\ts_nop 1" :: "v"(p), "v"(v) : "memory");
    else asm volatile("global_store_dwordx4 %0, %1, off sc0 sc1\n\ts_nop 1" :: "v"(p), "v"(v) : "memory");
}
#define POL1 2
#define POL2 0
#define POL3 2
#define MFMA16(a, b, c) __builtin_amdgcn_mfma_f32_16x16x32_bf16(a, b, c, 0, 0, 0)
#define BARRIER() do { asm volatile("" ::: "memory"); __builtin_amdgcn_s_waitcnt(0xC07F); __builtin_amdgcn_s_barrier(); asm volatile("" ::: "memory"); } while (0)
#define SBAR() __builtin_amdgcn_sched_barrier(0)
#define K1_RPM 1
template <int NM, int NR> DI void interleave() {
#pragma unroll
    for (int i = 0; i < NR / K1_RPM; ++i) { __builtin_amdgcn_sched_group_barrier(0x008, 1, 0); __builtin_amdgcn_sched_group_barrier(0x100, K1_RPM, 0); }
    __builtin_amdgcn_sched_group_barrier(0x008, NM - NR / K1_RPM, 0);
    SBAR();
}
#define STAMP_DECL
#define STAMP(kern, slot)
#define STAMP_DUMP(kern)
#define DIAG_DRAIN()

constexpr int A_PART = 6144, A_SLOT = 4 * A_PART, A_X = 3 * A_SLOT, A_LDS = A_X + 16 * 3072;
static_assert(4 * KT <= A_PART && 8 * VB <= A_PART && A_SLOT == 24576, "stage layout");

#define MFMA32(a, b, c) __builtin_amdgcn_mfma_f32_32x32x16_bf16(a, b, c, 0, 0, 0)
#define MFMA32R(a, b, c) __builtin_amdgcn_mfma_f32_32x32x8bf16_1k(a, b, c, 0, 0, 0)

DI void attn_exact(const char* kf, const char* vf, bf16x8 qm, bf16x4 qr, int lane, f32x16& o) {
    const int r32 = lane & 31, h = lane >> 5;
    float mrun = -3.0e38f;
#pragma unroll
    for (int r = 0; r < 16; ++r) o[r] = 0.f;
#pragma unroll 1
    for (int t = 0; t < 64; ++t) {
        const bf16x8 km = *(const bf16x8*)(kf + t * KT + lane * 16);
        const bf16x4 kr = *(const bf16x4*)(kf + t * KT + 1024 + r32 * 8);
        const int vo = r32 < 21 ? h * 336 + r32 * 16 : 672;
        const bf16x8 v0 = *(const bf16x8*)(vf + (2 * t) * VB + vo);
        const bf16x8 v1 = *(const bf16x8*)(vf + (2 * t + 1) * VB + vo);
        f32x16 s;
#pragma unroll
        for (int r = 0; r < 16; ++r) s[r] = 0.f;
        s = MFMA32(km, qm, s);
        s = MFMA32R(kr, qr, s);
        float mx = s[0];
#pragma unroll
        for (int r = 1; r < 16; ++r) mx = fmaxf(mx, s[r]);
        { auto rr = __builtin_amdgcn_permlane32_swap(__float_as_uint(mx), __float_as_uint(mx), false, false); mx = fmaxf(__uint_as_float(rr[0]), __uint_as_float(rr[1])); }
        const float mnew = fmaxf(mrun, mx), alpha = __builtin_amdgcn_exp2f(mrun - mnew);
        mrun = mnew;
        u32x4 p0, p1;
#pragma unroll
        for (int i = 0; i < 4; ++i) {
            p0[i] = cvtpk(__builtin_amdgcn_exp2f(s[2 * i] - mnew), __builtin_amdgcn_exp2f(s[2 * i + 1] - mnew));
            p1[i] = cvtpk(__builtin_amdgcn_exp2f(s[8 + 2 * i] - mnew), __builtin_amdgcn_exp2f(s[8 + 2 * i + 1] - mnew));
        }
#pragma unroll
        for (int r = 0; r < 16; ++r) o[r] *= alpha;
        o = MFMA32(v0, __builtin_bit_cast(bf16x8, p0), o);
        o = MFMA32(v1, __builtin_bit_cast(bf16x8, p1), o);
    }
}

DI float row_sum_of(const f32x16& o) {
    auto rr = __builtin_amdgcn_permlane32_swap(__float_as_uint(o[8]), __float_as_uint(o[8]), false, false);
    return __uint_as_float(rr[1]);
}

#define R_SA "v[64:79]"
#define R_SB "v[80:95]"
#define R_PL "v[100:103]"
#define R_PH "v[104:107]"
#define R_KM "v[108:111]"
#define R_KR "v[112:113]"
#define R_V0 "v[114:117]"
#define R_V1 "v[118:121]"
#define STR_(x) #x
#define STR(x) STR_(x)
#define MF16(d, a, b, c) "v_mfma_f32_32x32x16_bf16 " d ", " a ", " b ", " c "\n\t"
#define MF8(d, a, b, c) "v_mfma_f32_32x32x8_bf16 " d ", " a ", " b ", " c "\n\t"
#define CVT2(p0, p1) "v_cvt_pk_bf16_f32 v" STR(p0) ", v96, v97\n\tv_cvt_pk_bf16_f32 v" STR(p1) ", v98, v99\n\t"
#define EXP4(s0, s1, s2, s3) "v_exp_f32 v96, v" STR(s0) "\n\tv_exp_f32 v97, v" STR(s1) "\n\tv_exp_f32 v98, v" STR(s2) "\n\tv_exp_f32 v99, v" STR(s3) "\n\t"
#define GAP(p0, p1, s0, s1, s2, s3) CVT2(p0, p1) EXP4(s0, s1, s2, s3)
#define WAITL(n) "s_waitcnt lgkmcnt(" STR(n) ")\n\t"
#define RD128(d, a, off) "ds_read_b128 " d ", " a " offset:" STR(off) "\n\t"
#define RD64(d, a, off) "ds_read_b64 " d ", " a " offset:" STR(off) "\n\t"
#define TILE(MID, KOFF, V1OFF, V0OFF) \
    MF16(R_SB, R_KM, "%[qbm]", "0") RD128(R_KM, "v45", KOFF) GAP(106, 107, 64, 65, 66, 67) \
    MF8(R_SB, R_KR, "%[qbr]", R_SB) RD64(R_KR, "v46", KOFF) GAP(100, 101, 68, 69, 70, 71) \
    MF16("%[ob]", R_V1, R_PH, "%[ob]") RD128(R_V1, "v47", V1OFF) GAP(102, 103, 72, 73, 74, 75) \
    WAITL(3) MF16("%[oa]", R_V0, R_PL, "%[oa]") GAP(104, 105, 76, 77, 78, 79) \
    MID \
    WAITL(2) MF16(R_SA, R_KM, "%[qam]", "0") GAP(106, 107, 80, 81, 82, 83) \
    WAITL(1) MF8(R_SA, R_KR, "%[qar]", R_SA) GAP(100, 101, 84, 85, 86, 87) \
    WAITL(0) MF16("%[oa]", R_V1, R_PH, "%[oa]") GAP(102, 103, 88, 89, 90, 91) \
    MF16("%[ob]", R_V0, R_PL, "%[ob]") RD128(R_V0, "v47", V0OFF) GAP(104, 105, 92, 93, 94, 95)
#define TILE_LAST(MID, PRE_K, V1OFF, PRE_V0) \
    MF16(R_SB, R_KM, "%[qbm]", "0") PRE_K RD128(R_KM, "v45", 0) GAP(106, 107, 64, 65, 66, 67) \
    MF8(R_SB, R_KR, "%[qbr]", R_SB) RD64(R_KR, "v46", 0) GAP(100, 101, 68, 69, 70, 71) \
    MF16("%[ob]", R_V1, R_PH, "%[ob]") RD128(R_V1, "v47", V1OFF) GAP(102, 103, 72, 73, 74, 75) \
    WAITL(3) MF16("%[oa]", R_V0, R_PL, "%[oa]") GAP(104, 105, 76, 77, 78, 79) \
    MID \
    WAITL(2) MF16(R_SA, R_KM, "%[qam]", "0") GAP(106, 107, 80, 81, 82, 83) \
    WAITL(1) MF8(R_SA, R_KR, "%[qar]", R_SA) GAP(100, 101, 84, 85, 86, 87) \
    WAITL(0) MF16("%[oa]", R_V1, R_PH, "%[oa]") GAP(102, 103, 88, 89, 90, 91) \
    MF16("%[ob]", R_V0, R_PL, "%[ob]") PRE_V0 RD128(R_V0, "v47", 0) GAP(104, 105, 92, 93, 94, 95)

#define P0 2
#define P1 2
#define P2 1
#define P3 1
#define P4 0
#define P5 0
#define P6 3
#define P7 3
#define PRIO(n) "s_setprio " STR(n) "\n\t"
#define ASM_STAMP(r)
#define TOUCH_PF "global_load_lds_dword v42, %[pf]\n\t"
#define TOUCH_PO "global_load_lds_dword v41, %[po]\n\t"
#define DMA2(SLOTREG) \
    "s_cmp_ge_u32 %[wid], 8\n\ts_cbranch_scc1 5f\n\t" \
    "s_lshr_b32 s46, s45, 1\n\ts_add_u32 s46, s46, %[blk]\n\ts_and_b32 s46, s46, 3\n\ts_lshl_b32 s46, s46, 2\n\t" \
    "s_and_b32 s47, s45, 1\n\ts_add_u32 s46, s46, s47\n\ts_lshl_b32 s46, s46, 2\n\ts_add_u32 s45, s45, 1\n\t" \
    "s_mul_i32 s47, s46, 1296\n\ts_add_u32 s60, s48, s47\n\ts_addc_u32 s61, s49, 0\n\t" \
    "s_add_u32 m0, %[d0], " SLOTREG "\n\ts_mul_i32 s47, s46, %[p1]\n\tglobal_load_lds_dwordx4 v40, s[60:61]\n\t" \
    "s_add_u32 s60, s50, s47\n\ts_addc_u32 s61, s51, 0\n\t" \
    "s_add_u32 m0, m0, 0x2000\n\ts_mul_i32 s47, s46, 1376\n\tglobal_load_lds_dwordx4 v40, s[60:61]\n\t" \
    "s_add_u32 s60, s54, s47\n\ts_addc_u32 s61, s55, 0\n\t" \
    "s_add_u32 m0, m0, 0x2000\n\ts_nop 0\n\tglobal_load_lds_dwordx4 v40, s[60:61]\n\t" \
    "5:\n\t"

extern "C" __global__ __launch_bounds__(1024, 4) void attn_kernel(char* ws, const char* woA, const char* woB) {
    extern __shared__ __attribute__((aligned(16))) char smem[];
    const int tid = threadIdx.x, lane = tid & 63, wid = (int)rfl(tid >> 6);
    const int xcd = blockIdx.x & 7, idx = blockIdx.x >> 3;
    const int qblk = 2 * (xcd & 1) + (idx & 1), branch = idx >> 4, head = 8 * (xcd >> 1) + ((idx >> 1) & 7), kblk = 2 * (xcd & 1);
    const int kvh = branch ? 32 : head;
    const int qg = wid & 7, kh = wid >> 3;
    const unsigned kf_off = KF_OFF + kvh * KF_HEAD, vf_off = VF_OFF + kvh * VF_HEAD;
    const unsigned lds0 = (unsigned)(uintptr_t)smem;
    STAMP_DECL;
    STAMP(2, 0);
    const unsigned w8 = wid & 7, w1k = w8 * 1024;
    const unsigned src0 = kf_off + w1k + (w8 >= 6 ? 8 * KT - 6144 : 0);
    const unsigned src1 = w8 < 4 ? kf_off + 8 * KT + 2048 + w1k : vf_off + w1k - 4096, pitch1 = w8 < 4 ? KT : 2 * VB;
    const unsigned src2 = vf_off + w1k + (w8 < 2 ? 4096 : 16 * VB - 2048);
    const unsigned d0 = lds0 + wid * 1024;
    if (wid < 8) {
        const unsigned t0 = kblk * 16, l16 = lane * 16;
        glds16(ws + (src0 + t0 * KT), l16, d0); glds16(ws + (src1 + t0 * pitch1), l16, d0 + 8192); glds16(ws + (src2 + t0 * 2 * VB), l16, d0 + 16384);
    }
    __builtin_amdgcn_sched_barrier(0);
    const int qrow0 = qblk * 512 + qg * 64;
    const unsigned qoff = rfl(QF_OFF + (unsigned)(branch * 32 + head) * HEAD40 + (unsigned)qrow0 * 40);
    const unsigned kbase = rfl(lds0 + kh * A_PART), vbase = rfl(lds0 + 2 * A_PART + kh * A_PART);
    const char* pf; unsigned pfmax;
    { const char* code; CODE_ADDR(code, "outproj_kernel");
      pf = wid < 4 ? (xcd >> 2 ? woB : woA) + (idx * 200 + wid * 50) * 128 : code + (wid == 5 ? 8192 : 0); pfmax = wid < 4 ? 49 : 63; }
    const char* po = ws + OF_OFF + (size_t)((unsigned)(branch * 32 + head) * HEAD40 + (unsigned)(qrow0 + 32 * kh) * 40);
    bf16x8 qam, qbm; bf16x4 qar, qbr;
    f32x16 oa, ob;
    asm volatile(
        ASM_STAMP("s[72:73]")
        "v_lshlrev_b32 v40, 4, %[lane]\n\t"
        "s_mov_b64 s[52:53], %[ws]\n\t"
        "s_add_u32 s48, s52, %[src0]\n\ts_addc_u32 s49, s53, 0\n\ts_add_u32 s50, s52, %[src1]\n\ts_addc_u32 s51, s53, 0\n\ts_add_u32 s54, s52, %[src2]\n\ts_addc_u32 s55, s53, 0\n\t"
        "s_mov_b32 s45, 1\n\t"
        "v_and_b32 v41, 31, %[lane]\n\tv_lshrrev_b32 v42, 5, %[lane]\n\t"
        "s_add_u32 s62, s52, %[qoff]\n\ts_addc_u32 s63, s53, 0\n\t"
        "v_lshlrev_b32 v43, 3, v41\n\t"
        "v_mov_b64 %[qar], 0\n\tv_mov_b64 %[qbr], 0\n\t"
        "global_load_dwordx4 %[qam], v40, s[62:63]\n\tglobal_load_dwordx4 %[qbm], v40, s[62:63] offset:1280\n\t"
        "s_mov_b32 exec_hi, 0\n\t"
        "global_load_dwordx2 %[qar], v43, s[62:63] offset:1024\n\tglobal_load_dwordx2 %[qbr], v43, s[62:63] offset:2304\n\t"
        "s_mov_b32 exec_hi, -1\n\t"
        "v_cmp_eq_u32 vcc, 0, v42\n\t"
        "v_add_u32 v45, %[kbase], v40\n\t"
        "v_add_u32 v46, 0x400, v43\n\tv_mov_b32 v48, 0x500\n\ts_nop 0\n\t"
        "v_cndmask_b32 v46, v48, v46, vcc\n\tv_add_u32 v46, %[kbase], v46\n\t"
        "v_cmp_gt_u32 vcc, 21, v41\n\t"
        "v_mul_u32_u24 v47, 0x150, v42\n\tv_lshl_add_u32 v47, v41, 4, v47\n\tv_mov_b32 v48, 0x2a0\n\t"
        "v_cndmask_b32 v47, v48, v47, vcc\n\tv_add_u32 v47, %[vbase], v47\n\t"
        "v_mov_b32 v118, 0\n\tv_mov_b32 v119, 0\n\tv_mov_b32 v120, 0\n\tv_mov_b32 v121, 0\n\t"
        "v_mov_b32 v96, 0\n\tv_mov_b32 v97, 0\n\tv_mov_b32 v98, 0\n\tv_mov_b32 v99, 0\n\t"
        "v_mov_b32 v104, 0\n\tv_mov_b32 v105, 0\n\t"
        "v_mfma_f32_32x32x16_bf16 %[oa], v[118:121], v[118:121], 0\n\tv_mfma_f32_32x32x16_bf16 %[ob], v[118:121], v[118:121], 0\n\t"
        "s_mov_b32 s40, 0\n\t"
        "s_mov_b32 s41, 0\n\t"
        "s_mov_b32 s44, 24576\n\t"
        "s_waitcnt vmcnt(4)\n\t" ASM_STAMP("s[66:67]") "s_barrier\n\t" ASM_STAMP("s[68:69]")
        RD128(R_KM, "v45", 0) RD64(R_KR, "v46", 0) RD128(R_V0, "v47", 0)
        "s_waitcnt vmcnt(0)\n\t"
        DMA2("s44")
        WAITL(0)
        MF16(R_SA, R_KM, "%[qam]", "0") MF8(R_SA, R_KR, "%[qar]", R_SA)
        "s_nop 15\n\ts_nop 3\n\t"
        ASM_STAMP("s[70:71]")
        "2:\n\t"
        "s_add_u32 s42, s41, 24576\n\ts_cmp_eq_u32 s42, 73728\n\ts_cselect_b32 s42, 0, s42\n\t"
        "s_sub_u32 s43, s42, s41\n\t"
        "s_add_u32 s44, s42, 24576\n\ts_cmp_eq_u32 s44, 73728\n\ts_cselect_b32 s44, 0, s44\n\t"
        PRIO(P0) TILE(PRIO(P1), 1296, 688, 1376)
        PRIO(P2) TILE(PRIO(P3), 2592, 2064, 2752)
        PRIO(P4) TILE(PRIO(P5), 3888, 3440, 4128)
        PRIO(P6) TILE_LAST(PRIO(P7),
            "s_waitcnt vmcnt(0)\n\ts_barrier\n\t"
            "v_add_u32 v45, s43, v45\n\tv_add_u32 v46, s43, v46\n\t"
            "s_cmp_ge_u32 s40, 6\n\ts_cbranch_scc1 3f\n\t"
            DMA2("s44")
            "s_branch 4f\n\t"
            "3:\n\t"
            "s_cmp_lg_u32 s40, 6\n\ts_cbranch_scc1 4f\n\t"
            "v_lshrrev_b32 v41, 4, v40\n\tv_min_u32 v42, %[pfmax], v41\n\tv_min_u32 v41, 9, v41\n\tv_lshlrev_b32 v42, 7, v42\n\tv_lshlrev_b32 v41, 7, v41\n\t"
            "s_add_u32 m0, %[d0], s44\n\ts_nop 0\n\t"
            TOUCH_PF TOUCH_PO
            "4:\n\t",
            4816, "v_add_u32 v47, s43, v47\n\t")
        "s_mov_b32 s41, s42\n\t"
        "s_add_u32 s40, s40, 1\n\t"
        "s_cmp_lt_u32 s40, 8\n\ts_cbranch_scc1 2b\n\t"
        "s_nop 1\n\t" CVT2(106, 107) "s_nop 1\n\t"
        MF16("%[ob]", R_V1, R_PH, "%[ob]")
        "s_waitcnt lgkmcnt(0)\n\ts_nop 15\n\ts_nop 3\n\t"
        : [oa] "=&v"(oa), [ob] "=&v"(ob), [qam] "=&v"(qam), [qbm] "=&v"(qbm), [qar] "=&v"(qar), [qbr] "=&v"(qbr)
        : [lane] "v"(lane), [ws] "s"(ws), [src0] "s"(src0), [src1] "s"(src1), [src2] "s"(src2), [p1] "s"(pitch1), [blk] "s"(kblk), [qoff] "s"(qoff),
          [kbase] "s"(kbase), [vbase] "s"(vbase), [d0] "s"(d0), [wid] "s"(wid), [pf] "s"(pf), [po] "s"(po), [pfmax] "s"(pfmax)
        : "memory", "scc", "vcc", "s40", "s41", "s42", "s43", "s44", "s45", "s46", "s47", "s48", "s49", "s50", "s51", "s52", "s53", "s54", "s55", "s60", "s61", "s62", "s63",
          "s64", "s65", "s66", "s67", "s68", "s69", "s70", "s71", "s72", "s73",
          "v40", "v41", "v42", "v43", "v44", "v45", "v46", "v47", "v48",
          "v64", "v65", "v66", "v67", "v68", "v69", "v70", "v71", "v72", "v73", "v74", "v75", "v76", "v77", "v78", "v79",
          "v80", "v81", "v82", "v83", "v84", "v85", "v86", "v87", "v88", "v89", "v90", "v91", "v92", "v93", "v94", "v95",
          "v96", "v97", "v98", "v99", "v100", "v101", "v102", "v103", "v104", "v105", "v106", "v107", "v108", "v109", "v110", "v111",
          "v112", "v113", "v114", "v115", "v116", "v117", "v118", "v119", "v120", "v121");
    STAMP(2, 2);
    const int r32 = lane & 31, h = lane >> 5;
    f32x16 o;
    {
        lds_ptr xb = (lds_ptr)smem + A_X + wid * 3072 + lane * 16;
        if (kh) {
#pragma unroll
            for (int i = 0; i < 3; ++i) *(__attribute__((address_space(3))) f32x4*)(xb + i * 1024) = f32x4{oa[4 * i], oa[4 * i + 1], oa[4 * i + 2], oa[4 * i + 3]};
            o = ob;
        } else {
#pragma unroll
            for (int i = 0; i < 3; ++i) *(__attribute__((address_space(3))) f32x4*)(xb + i * 1024) = f32x4{ob[4 * i], ob[4 * i + 1], ob[4 * i + 2], ob[4 * i + 3]};
            o = oa;
        }
    }
    BARRIER();
    {
        lds_cptr xb = (lds_cptr)smem + A_X + (wid ^ 8) * 3072 + lane * 16;
#pragma unroll
        for (int i = 0; i < 3; ++i) { const f32x4 v = *(const __attribute__((address_space(3))) f32x4*)(xb + i * 1024);
#pragma unroll
            for (int e = 0; e < 4; ++e) o[4 * i + e] += v[e]; }
    }
    float l = row_sum_of(o);
    float mag = 0.f;
#pragma unroll
    for (int r = 0; r < 12; ++r) mag += fabsf(o[r]);
    const bool ok = l >= 0x1p-60f && l <= 0x1p60f && mag <= 3.0e38f;
    if (__builtin_expect(!__all(ok), 0)) {
        attn_exact(ws + kf_off, ws + vf_off, kh ? qbm : qam, kh ? qbr : qar, lane, o);
        l = row_sum_of(o);
    }
    const float inv = __builtin_amdgcn_rcpf(l);
    {
        lds_ptr so = (lds_ptr)smem + A_X + (wid ^ 8) * 3072;
        lds_st64(so + r32 * 40 + 8 * h, u32x2{cvtpk(o[0] * inv, o[1] * inv), cvtpk(o[2] * inv, o[3] * inv)});
        lds_st64(so + r32 * 40 + 16 + 8 * h, u32x2{cvtpk(o[4] * inv, o[5] * inv), cvtpk(o[6] * inv, o[7] * inv)});
        if (h == 0) lds_st64(so + r32 * 40 + 32, u32x2{cvtpk(o[8] * inv, o[9] * inv), cvtpk(o[10] * inv, o[11] * inv)});
        asm volatile("" ::: "memory"); __builtin_amdgcn_s_waitcnt(0xC07F); asm volatile("" ::: "memory");
        char* od = ws + OF_OFF + (size_t)((unsigned)(branch * 32 + head) * HEAD40 + (unsigned)(qrow0 + 32 * kh) * 40) + lane * 16;
        st128<POL2>(od, lds_b128((lds_cptr)so + lane * 16));
        if (lane < 16) st128<POL2>(od + 1024, lds_b128((lds_cptr)so + 1024 + lane * 16));
    }
    DIAG_DRAIN();
    STAMP(2, 7);
    STAMP_DUMP(2);
}

#define K3_RPM 2
constexpr int K3_X = 0, K3_W = 32 * 3840, K3_LDS = K3_W + 4 * 8192;
constexpr int k3_total(int t) { int n = 14; for (int u = 0; u < t; ++u) n += (u <= 16 ? 2 : 0) + (u <= 10 ? 2 : 0); return n; }
constexpr int k3_allowed(int t) {
    int last = t <= 2 ? 10 + 2 * t : k3_total(t - 3) + 2;
    const int xg = t == 5 ? 3 : t == 10 ? 7 : t == 15 ? 10 : -1;
    if (xg >= 0) { const int e = k3_total(xg) + 4; if (e > last) last = e; }
    return k3_total(t) - last;
}
DI int k3_krow(int c, int e) { const int P = c / 5, r = c - 5 * P; return r < 4 ? (2 * P + (r >> 1)) * 20 + (r & 1) * 8 + e : (2 * P + (e >> 2)) * 20 + 16 + (e & 3); }

DI unsigned k3_xoff(int o, int rot) { const int hd = o / 3840; return (unsigned)(((hd + 8 * rot) & 31) * HEAD40 + (o - hd * 3840)); }
template <int T> DI void k3_load_steps(const char* xsrc, const char* wsrc, const unsigned (&wo)[5][2], int lw, int lane, unsigned lds0, int rot) {
    if constexpr (T < 20) {
        wait_vm<k3_allowed(T)>();
        asm volatile("s_barrier" ::: "memory");
        if constexpr (T + 3 < 20) {
            constexpr int t = T + 3;
#pragma unroll
            for (int i = 0; i < 2; ++i) glds16(wsrc + (size_t)((t / 5 + rot) & 3) * 160 * (DM * 2), wo[t % 5][i], lds0 + K3_W + (t & 3) * 8192 + (2 * lw + i) * 1024);
        }
        if constexpr (T <= 10) {
#pragma unroll
            for (int i = 0; i < 2; ++i) { const int pc = lw + 4 * (8 + 2 * T + i); glds16(xsrc, k3_xoff(pc * 1024 + lane * 16, rot), lds0 + K3_X + pc * 1024); }
        }
        k3_load_steps<T + 1>(xsrc, wsrc, wo, lw, lane, lds0, rot);
    }
}

extern "C" __global__ __launch_bounds__(512, 2) void outproj_kernel(char* ws, const char* woA, const char* woB, char* out) {
    extern __shared__ __attribute__((aligned(16))) char smem[];
    const int tid = threadIdx.x, lane = tid & 63, wid = (int)rfl(tid >> 6);
    const int xcd = blockIdx.x & 7, idx = blockIdx.x >> 3, br = xcd >> 2;
    const int blk = 2 * (xcd & 1) + ((xcd >> 1) & 1), rot = xcd >> 1;
    if (idx >= 30) return;
    STAMP_DECL;
    const int rl = idx / 5, ct = idx - 5 * rl, row0 = blk * 512 + (rl < 5 ? rl * 96 : 416);
    if (wid >= 4) {
        const int lw = wid - 4;
        const char* xsrc = ws + OF_OFF + (size_t)br * 32 * HEAD40 + row0 * 40;
        const char* wsrc = (br ? woB : woA) + ct * 256;
        const unsigned lds0 = (unsigned)(uintptr_t)smem;
#pragma unroll
        for (int j = 0; j < 8; ++j) { const int pc = lw + 4 * j; glds16(xsrc, k3_xoff(pc * 1024 + lane * 16, rot), lds0 + K3_X + pc * 1024); }
        SBAR();
        unsigned wo[5][2];
#pragma unroll
        for (int tt = 0; tt < 5; ++tt)
#pragma unroll
            for (int i = 0; i < 2; ++i) {
                const int rho = 4 * (2 * lw + i) + (lane >> 4), s16 = lane & 15, kap = swap23(rho);
                wo[tt][i] = (unsigned)(k3_krow(4 * tt + (kap >> 3), kap & 7) * (DM * 2) + (((s16 >> 1) ^ (rho & 7)) << 5) + (s16 & 1) * 16);
            }
#pragma unroll
        for (int t = 0; t < 3; ++t)
#pragma unroll
            for (int i = 0; i < 2; ++i) glds16(wsrc + (size_t)rot * 160 * (DM * 2), wo[t][i], lds0 + K3_W + t * 8192 + (2 * lw + i) * 1024);
        k3_load_steps<0>(xsrc, wsrc, wo, lw, lane, lds0, rot);
        asm volatile("s_barrier\n\ts_barrier" ::: "memory");
    } else {
        lds_cptr lds = (lds_cptr)smem;
        const int wm = wid >> 1, wn = wid & 1, m = lane & 15, g = lane >> 4, q = (lane & 15) >> 2, p = lane & 3;
        unsigned xlo[5], xhi[5], wa[4];
#pragma unroll
        for (int tt = 0; tt < 5; ++tt) {
            const int c = 4 * tt + g, P = c / 5, r = c - 5 * P, rb = (48 * wm + m) * 40;
            xlo[tt] = (unsigned)(K3_X + rb + (r < 4 ? (2 * P + (r >> 1)) * 3840 + (r & 1) * 16 : 2 * P * 3840 + 32));
            xhi[tt] = (unsigned)(K3_X + rb + (r < 4 ? (2 * P + (r >> 1)) * 3840 + (r & 1) * 16 + 8 : (2 * P + 1) * 3840 + 32));
        }
        { const int sx = (g & 1) * 4 + q;
#pragma unroll
          for (int j = 0; j < 4; ++j) wa[j] = (unsigned)(K3_W + ((g >> 1) * 16 + sx) * 256 + (((4 * wn + j) ^ sx) << 5) + p * 8); }
        f32x4 acc[3][4];
#pragma unroll
        for (int i = 0; i < 3; ++i)
#pragma unroll
            for (int j = 0; j < 4; ++j) acc[i][j] = f32x4{0.f, 0.f, 0.f, 0.f};
        bf16x8 xf[2][3], wf[2][4];
        auto load = [&](int t) {
            const int tt = t % 5, u = t / 5;
#pragma unroll
            for (int i = 0; i < 3; ++i) xf[t & 1][i] = cat8(lds_b64(lds + xlo[tt] + u * 30720 + i * 640), lds_b64(lds + xhi[tt] + u * 30720 + i * 640));
#pragma unroll
            for (int j = 0; j < 4; ++j) wf[t & 1][j] = cat8(lds_tr(lds + wa[j] + (t & 3) * 8192), lds_tr(lds + wa[j] + (t & 3) * 8192 + 2048));
        };
        STAMP(3, 0);
        BARRIER();
        STAMP(3, 1);
        load(0);
        SBAR();
#pragma unroll
        for (int t = 0; t < 20; ++t) {
            if (t + 1 < 20) { BARRIER(); load(t + 1); }
#pragma unroll
            for (int i = 0; i < 3; ++i)
#pragma unroll
                for (int j = 0; j < 4; ++j) acc[i][j] = MFMA16(wf[t & 1][j], xf[t & 1][i], acc[i][j]);
            if (t + 1 < 20) {
#pragma unroll
                for (int i = 0; i < 14 / K3_RPM; ++i) { __builtin_amdgcn_sched_group_barrier(0x100, K3_RPM, 0); __builtin_amdgcn_sched_group_barrier(0x008, 1, 0); }
                __builtin_amdgcn_sched_group_barrier(0x100, 14, 0); __builtin_amdgcn_sched_group_barrier(0x008, 12, 0);
            }
            SBAR();
        }
        STAMP(3, 2);
        BARRIER();
        STAMP(3, 3);
        lds_ptr st = (lds_ptr)smem;
#pragma unroll
        for (int i = 0; i < 3; ++i) {
            const int row = 48 * wm + 16 * i + m;
#pragma unroll
            for (int j = 0; j < 4; ++j) { const int b = (64 * wn + 16 * j + 4 * g) * 2; lds_st64(st + row * 256 + ((((b >> 4) ^ (row & 15)) << 4) | (b & 8)), pack4(acc[i][j])); }
        }
        BARRIER();
        STAMP(3, 4);
    }
#pragma unroll
    for (int it = 0; it < 3; ++it) {
        const int o = it * 8192 + tid * 16, row = o >> 8, s = (o >> 4) & 15;
        st128<POL3>(out + (size_t)(row0 + row) * 2560 + br * 1280 + ct * 256 + ((s ^ (row & 15)) << 4), lds_b128((lds_cptr)smem + o));
    }
    if (wid < 4) { STAMP(3, 5); DIAG_DRAIN(); STAMP(3, 7); STAMP_DUMP(3); }
}

constexpr int NSTAGE = DM / 64;
constexpr int K1_X = 0, K1_W = 16384, K1_W2 = K1_W + 24576, K1_SLOT = K1_W2 + 3072, K1_LDS = 3 * K1_SLOT + 256;

template <int NP> DI void k1_loader(const char* xg, const char* wg, const char* w2g, int wpitch, int lw, int lane, unsigned lds0, const char* tdst, unsigned dstride) {
    unsigned xo[4], wo[6], w2o = 0;
#pragma unroll
    for (int i = 0; i < 4; ++i) { const int row = (4 * lw + i) * 8 + (lane >> 3), pos = lane & 7; xo[i] = (unsigned)(row * (DM * 2) + ((pos ^ ((row >> 1) & 7)) << 4)); }
#pragma unroll
    for (int j = 0; j < 3; ++j) {
        const int o = j * 1024 + lane * 16, r = (o * 171) >> 16, b = o - r * 384;
        wo[j] = (unsigned)((16 * lw + 8 * (r >> 2) + (r & 3)) * wpitch + (((b >> 5) ^ ((r >> 1) & 3)) << 5) + (b & 16));
        wo[j + 3] = wo[j] + 4u * (unsigned)wpitch;
    }
    const int p2 = lw;
    if (NP == 11) { const int o = p2 * 1024 + lane * 16; w2o = (unsigned)(o < 2560 ? o : 2544); }
    auto issue_w = [&](int s, int slot) {
        const unsigned sl = lds0 + (unsigned)slot * K1_SLOT;
        const char* wsrc = wg + (size_t)s * 64 * wpitch;
#pragma unroll
        for (int i = 0; i < 6; ++i) glds16(wsrc, wo[i], sl + K1_W + (6 * lw + i) * 1024);
        if (NP == 11) glds16(w2g + (size_t)s * 2560, w2o, sl + K1_W2 + p2 * 1024);
    };
    auto issue = [&](int s, int slot) {
        const unsigned sl = lds0 + (unsigned)slot * K1_SLOT;
        const char* xs = xg + (size_t)s * 128;
#pragma unroll
        for (int i = 0; i < 4; ++i) glds16(xs, xo[i], sl + K1_X + (4 * lw + i) * 1024);
        issue_w(s, slot);
    };
    issue_w(0, 0); issue(1, 1);
    int nslot = 2;
#pragma unroll 1
    for (int s = 0; s < NSTAGE; ++s) {
        if (s < NSTAGE - 1) wait_vm<NP>(); else wait_vm<0>();
        asm volatile("s_barrier" ::: "memory");
        if (s + 2 < NSTAGE) { issue(s + 2, nslot); nslot = nslot == 2 ? 0 : nslot + 1; }
    }
}

#define TOUCH_ALL 0
struct K1Frag { bf16x8 x[4]; bf16x8 w[5]; bf16x8 w2; };

#define STP_PARAM
#define STP_ARG
template <int KIND, int XTRA> DI void k1_consumer(char* ws, char* smem, int wid, int lane, int shift32, int xo, int row0, const char* ahead STP_PARAM) {
    lds_cptr lds = (lds_cptr)smem;
    const int wm = wid >> 1, wn = wid & 1, m = lane & 15, g = lane >> 4, q = (lane & 15) >> 2, p = lane & 3;
    const unsigned xa0 = K1_X + (unsigned)((64 * wm + m) * 128 + ((g ^ ((m >> 1) & 7)) << 4));
    unsigned wa[5];
    { const int gx = ((g & 1) << 1) | (q >> 1);
#pragma unroll
      for (int j = 0; j < 5; ++j) wa[j] = K1_W + (unsigned)(((g >> 1) * 16 + (g & 1) * 4 + q) * 384 + (((shift32 + 5 * wn + j) ^ gx) << 5) + p * 8); }
    const unsigned w2a = K1_W2 + (unsigned)((8 * g + q) * 40 + p * 8 + xo * 2);
    f32x4 acc[4][5], acc2[2];
#pragma unroll
    for (int i = 0; i < 4; ++i)
#pragma unroll
        for (int j = 0; j < 5; ++j) acc[i][j] = f32x4{0.f, 0.f, 0.f, 0.f};
    acc2[0] = acc2[1] = f32x4{0.f, 0.f, 0.f, 0.f};
    auto load = [&](K1Frag& f, unsigned sl, int kk) {
        lds_cptr b = lds + sl;
#pragma unroll
        for (int i = 0; i < 4; ++i) f.x[i] = lds_b128(b + ((xa0 ^ (kk * 64)) + i * 2048));
#pragma unroll
        for (int j = 0; j < 5; ++j) f.w[j] = cat8(lds_tr(b + wa[j] + kk * 12288), lds_tr(b + wa[j] + kk * 12288 + 3072));
        if (XTRA) f.w2 = cat8(lds_tr(b + w2a + kk * 1280), lds_tr(b + w2a + kk * 1280 + 160));
    };
    auto mma = [&](const K1Frag& f) {
#pragma unroll
        for (int i = 0; i < 4; ++i)
#pragma unroll
            for (int j = 0; j < 5; ++j) acc[i][j] = KIND == 2 ? MFMA16(f.x[i], f.w[j], acc[i][j]) : MFMA16(f.w[j], f.x[i], acc[i][j]);
        if (XTRA) {
#pragma unroll
            for (int i = 0; i < 2; ++i) { const bf16x8 xx = wn ? f.x[2 + i] : f.x[i]; acc2[i] = XTRA == 2 ? MFMA16(xx, f.w2, acc2[i]) : MFMA16(f.w2, xx, acc2[i]); }
        }
    };
    constexpr int CH = KIND == 0 ? 5120 : KIND == 1 ? 4 * KT : 8 * VB, TP = KIND == 0 ? 1280 : KT;
    unsigned sa[5], sh[5];
#pragma unroll
    for (int j = 0; j < 5; ++j) {
        const int col = 80 * wn + 16 * j + (KIND == 2 ? m : 4 * g), hl = (col * 205) >> 12, d = col - hl * 20;
        if (KIND == 2) { sa[j] = (unsigned)(hl * CH + 4 * wm * VB + (g & 1) * 336 + (g >> 1) * 8 + d * 16); sh[j] = 0; }
        else { const bool rem = d == 16; sa[j] = (unsigned)(hl * CH + 2 * wm * TP + (rem ? 1024 + m * 8 : (d >> 3) * 512 + m * 16 + (d & 4) * 2)); sh[j] = sa[j] + (rem ? 128 : 256); asm volatile("" : "+v"(sh[j])); }
        asm volatile("" : "+v"(sa[j]));
    }
    K1Frag f0, f1;
    STAMP(1, 0);
    BARRIER();
    STAMP(1, 1);
    const unsigned scratch = rfl((unsigned)(uintptr_t)smem + 3 * K1_SLOT);
    load(f0, 0, 0);
    int slot = 0;
    constexpr int NM = XTRA ? 22 : 20, NR = XTRA ? 16 : 14;
    SBAR();
#pragma unroll 1
    for (int s = 0; s < NSTAGE - 1; ++s) {
        load(f1, (unsigned)slot * K1_SLOT, 1);
        mma(f0);
        interleave<NM, NR>();
        slot = slot == 2 ? 0 : slot + 1;
        BARRIER();
        if (wid == (s & 3)) {
            if (s == 8) { const char* code; CODE_ADDR(code, "attn_kernel"); touch_lines(code, lane * 128, scratch); }
            SBAR();
        }
        load(f0, (unsigned)slot * K1_SLOT, 0);
        mma(f1);
        interleave<NM, NR>();
    }
    load(f1, (unsigned)slot * K1_SLOT, 1);
    mma(f0);
    interleave<NM, NR>();
    mma(f1);
    STAMP(1, 2);
    BARRIER();
    STAMP(1, 3);
    lds_ptr st = (lds_ptr)smem;
#pragma unroll
    for (int j = 0; j < 5; ++j) {
        if (KIND == 2) {
#pragma unroll
            for (int i = 0; i < 4; ++i) lds_st64(st + sa[j] + i * VB, pack4(acc[i][j]));
        } else {
            const float sc = KIND == 0 ? QSCALE : 1.0f;
            lds_st64(st + sa[j], pack4(acc[0][j] * sc)); lds_st64(st + sa[j] + TP, pack4(acc[2][j] * sc));
            lds_st64(st + sh[j], pack4(acc[1][j] * sc)); lds_st64(st + sh[j] + TP, pack4(acc[3][j] * sc));
        }
    }
    if (XTRA == 1) {
        const int d0 = xo + 4 * g;
        if (d0 < (xo ? 20 : 12)) {
#pragma unroll
            for (int i = 0; i < 2; ++i) {
                const int row = row0 + 64 * wm + 32 * wn + 16 * i + m, key = row & 31;
                *(u32x2*)(ws + KF_OFF + (size_t)32 * KF_HEAD + (row >> 5) * KT + (d0 < 16 ? (d0 >> 3) * 512 + key * 16 + (d0 & 4) * 2 : 1024 + key * 8)) = pack4(acc2[i]);
            }
        }
        if (xo == 0 && lane == 0 && wn == 0) {
#pragma unroll
            for (int i = 0; i < 2; ++i) *(u32x4*)(ws + KF_OFF + (size_t)32 * KF_HEAD + ((row0 >> 5) + 2 * wm + i) * KT + 1280) = u32x4{0u, 0u, 0u, 0u};
        }
    }
    if (XTRA == 2) {
        const int d = xo + m;
#pragma unroll
        for (int i = 0; i < 2; ++i) {
            char* dst = ws + VF_OFF + (size_t)32 * VF_HEAD + ((row0 >> 4) + 4 * wm + 2 * wn + i) * VB;
            if (d < (xo ? 20 : 12)) *(u32x2*)(dst + (g & 1) * 336 + (g >> 1) * 8 + d * 16) = pack4(acc2[i]);
            if (xo == 0 && m == 15) *(u32x2*)(dst + (g & 1) * 336 + (g >> 1) * 8 + 320) = u32x2{0x3F803F80u, 0x3F803F80u};
            if (xo == 0 && lane == 14) *(u32x4*)(dst + 672) = u32x4{0u, 0u, 0u, 0u};
        }
    }
    BARRIER();
    STAMP(1, 4);
}

template <int CH> DI void k1_copy_out(const char* smem, char* dst, unsigned stride, int wid, int lane) {
    lds_cptr src = (lds_cptr)smem + wid * CH + lane * 16;
    char* d = dst + (size_t)wid * stride + lane * 16;
    bf16x8 v[5];
#pragma unroll
    for (int i = 0; i < 5; ++i) v[i] = lds_b128(src + i * 1024);
#pragma unroll
    for (int i = 0; i < 5; ++i) st128<POL1>(d + i * 1024, v[i]);
    if (CH > 5120 && lane < (CH - 5120) / 16) st128<POL1>(d + 5120, lds_b128(src + 5120));
}

extern "C" __global__ __launch_bounds__(512, 2) void proj_kernel(const char* xA, const char* wqkv, const char* xB, const char* wq, const char* wk, const char* wv, char* ws) {
    extern __shared__ __attribute__((aligned(16))) char smem[];
    const int tid = threadIdx.x, lane = tid & 63, wid = (int)rfl(tid >> 6);
    {
        const char* code; CODE_ADDR(code, "proj_kernel");
        if (wid < 3) touch_lines(code + wid * 8192, (wid == 2 ? (lane < 24 ? lane : 23) : lane) * 128, rfl((unsigned)(uintptr_t)smem + 3 * K1_SLOT));
        __builtin_amdgcn_sched_barrier(0);
    }
#define MAP1 0
    const int xcd = blockIdx.x & 7, idx = blockIdx.x >> 3, blk = xcd & 3, hh = xcd >> 2, grp = idx >> 3;
    const bool isB = grp == 1;
    const int rt = 4 * blk + (idx & 3);
    const int ct = (grp < 2 ? 0 : 4 * (grp - 1)) + 2 * hh + ((idx >> 2) & 1);
    if (wid >= 4) {
        const char* xg0 = (isB ? xB : xA) + (size_t)rt * 128 * (DM * 2);
#pragma unroll
        for (int i = 0; i < 4; ++i) { const int row = (4 * (wid - 4) + i) * 8 + (lane >> 3); glds16(xg0, (unsigned)(row * (DM * 2) + (((lane & 7) ^ ((row >> 1) & 7)) << 4)), (unsigned)(uintptr_t)smem + K1_X + (4 * (wid - 4) + i) * 1024); }
    }
    __builtin_amdgcn_sched_barrier(0);
    STAMP_DECL;
    const int kind = isB ? 0 : ct >> 2, head0 = 8 * (ct & 3), row0 = rt * 128;
    char* dst = kind == 2 ? ws + VF_OFF + (size_t)head0 * VF_HEAD + (row0 >> 4) * VB : kind == 1 ? ws + KF_OFF + (size_t)head0 * KF_HEAD + (row0 >> 5) * KT : ws + QF_OFF + (size_t)((isB ? 32 : 0) + head0) * HEAD40 + row0 * 40;
    const unsigned dstride = kind == 2 ? VF_HEAD : kind == 1 ? KF_HEAD : HEAD40;
    if (wid >= 4) {
        const char* xg = (isB ? xB : xA) + (size_t)row0 * (DM * 2);
        const int wpitch = isB ? DM * 2 : 3 * DM * 2;
        const char* wg = (isB ? wq : wqkv) + ((ct * 320) & ~127);
        const char* w2g = ct >= 2 ? wv : wk;
        const unsigned lds0 = (unsigned)(uintptr_t)smem;
        const char* tdst = (kind == 0 || (row0 & 128) == 0) ? (const char*)((uintptr_t)dst & ~(uintptr_t)127) : nullptr;
        if (isB && wid < 7) k1_loader<11>(xg, wg, w2g, wpitch, wid - 4, lane, lds0, tdst, dstride); else k1_loader<10>(xg, wg, w2g, wpitch, wid - 4, lane, lds0, tdst, dstride);
        asm volatile("s_barrier" ::: "memory");
        { lds_ptr st = (lds_ptr)smem; const int t = tid - 256;
          if (kind == 1 && t < 32) *(__attribute__((address_space(3))) u32x4*)(st + t * KT + 1280) = u32x4{0u, 0u, 0u, 0u};
          if (kind == 2) {
              if (t < 64) *(__attribute__((address_space(3))) u32x4*)(st + t * VB + 672) = u32x4{0u, 0u, 0u, 0u};
              if (t >= 64 && t < 192) *(__attribute__((address_space(3))) u32x4*)(st + ((t - 64) >> 1) * VB + (t & 1) * 336 + 320) = u32x4{0x3F803F80u, 0x3F803F80u, 0x3F803F80u, 0x3F803F80u};
          } }
        BARRIER();
    } else {
        const char* ahead;
        {
            const int bb = blockIdx.x, j0 = lane & 31, j = (j0 < 27 || bb < 8) ? j0 : 26, s = 2 + 2 * wid + (lane >> 5);
            if (j < 16) ahead = (j < 8 ? xA : xB) + (size_t)(8 * bb + (j & 7)) * (DM * 2) + s * 128;
            else if (j < 24) { int n = 8 * bb + j - 16; n = n < 1920 ? n : 1919; const int r = n / 30; ahead = wqkv + (size_t)(64 * s + r) * (3 * DM * 2) + (n - 30 * r) * 128; }
            else if (j < 27) { int n = 3 * bb + j - 24; n = n < 640 ? n : 639; const int r = n / 10; ahead = wq + (size_t)(64 * s + r) * (DM * 2) + (n - 10 * r) * 128; }
            else { const int n = 5 * bb + j - 27; ahead = (n < 20 ? wk + n * 128 : wv + (n - 20) * 128) + s * 2560; }
            touch_addr(ahead, rfl((unsigned)(uintptr_t)smem + 3 * K1_SLOT));
        }
        const int shift32 = (ct & 1) * 2, xo = (ct & 1) * 12;
        if (isB) { if (ct < 2) k1_consumer<0, 1>(ws, smem, wid, lane, shift32, xo, row0, ahead STP_ARG); else k1_consumer<0, 2>(ws, smem, wid, lane, shift32, xo, row0, ahead STP_ARG); }
        else if (kind == 2) k1_consumer<2, 0>(ws, smem, wid, lane, shift32, 0, row0, ahead STP_ARG);
        else if (kind == 1) k1_consumer<1, 0>(ws, smem, wid, lane, shift32, 0, row0, ahead STP_ARG);
        else k1_consumer<0, 0>(ws, smem, wid, lane, shift32, 0, row0, ahead STP_ARG);
    }
    if (kind == 2) k1_copy_out<8 * VB>(smem, dst, dstride, wid, lane);
    else if (kind == 1) k1_copy_out<4 * KT>(smem, dst, dstride, wid, lane);
    else k1_copy_out<5120>(smem, dst, dstride, wid, lane);
    if (wid < 4) { STAMP(1, 5); DIAG_DRAIN(); STAMP(1, 7); STAMP_DUMP(1); }
}

extern "C" void kernel_launch(void* const* d_in, const int* in_sizes, int n_in, void* d_out, int out_size, void* d_ws, size_t ws_size, hipStream_t stream) {
    (void)in_sizes; (void)n_in; (void)out_size; (void)ws_size;
    const char* xA = (const char*)d_in[0]; const char* wqkv = (const char*)d_in[1]; const char* woA = (const char*)d_in[2];
    const char* xB = (const char*)d_in[3]; const char* wq = (const char*)d_in[4]; const char* wk = (const char*)d_in[5]; const char* wv = (const char*)d_in[6]; const char* woB = (const char*)d_in[7];
    char* ws = (char*)d_ws;
    (void)hipFuncSetAttribute((const void*)proj_kernel, hipFuncAttributeMaxDynamicSharedMemorySize, K1_LDS);
    (void)hipFuncSetAttribute((const void*)attn_kernel, hipFuncAttributeMaxDynamicSharedMemorySize, A_LDS);
    (void)hipFuncSetAttribute((const void*)outproj_kernel, hipFuncAttributeMaxDynamicSharedMemorySize, K3_LDS + 256);
    proj_kernel<<<256, 512, K1_LDS, stream>>>(xA, wqkv, xB, wq, wk, wv, ws);
    attn_kernel<<<256, 1024, A_LDS, stream>>>(ws, woA, woB);
    outproj_kernel<<<240, 512, K3_LDS + 256, stream>>>(ws, woA, woB, (char*)d_out);
}
```

```cpp
#include <hip/hip_runtime.h>
#include <stdint.h>

typedef short bf16x8 __attribute__((ext_vector_type(8)));
typedef short bf16x4 __attribute__((ext_vector_type(4)));
typedef float f32x2 __attribute__((ext_vector_type(2)));
typedef float f32x4 __attribute__((ext_vector_type(4)));
typedef float f32x16 __attribute__((ext_vector_type(16)));
typedef unsigned u32x2 __attribute__((ext_vector_type(2)));
typedef unsigned u32x4 __attribute__((ext_vector_type(4)));
typedef __bf16 bf16x2_t __attribute__((ext_vector_type(2)));
typedef short v4i16_t __attribute__((ext_vector_type(4)));
typedef __attribute__((address_space(3))) const char* lds_cptr;
typedef __attribute__((address_space(3))) char* lds_ptr;

#define DI __device__ __forceinline__
#define MAP2 2

constexpr int SEQ = 2048, DM = 640;
constexpr float QSCALE = 0.22360679774997896f * 1.4426950408889634f;

constexpr unsigned HEAD40 = SEQ * 40, KT = 1296, VB = 688, KF_HEAD = 64 * KT, VF_HEAD = 128 * VB;
constexpr unsigned QF_OFF = 0, KF_OFF = QF_OFF + 64 * HEAD40, VF_OFF = KF_OFF + 33 * KF_HEAD, OF_OFF = VF_OFF + 33 * VF_HEAD + 2048;

DI unsigned cvtpk(float lo, float hi) { f32x2 v = {lo, hi}; bf16x2_t b = __builtin_convertvector(v, bf16x2_t); return __builtin_bit_cast(unsigned, b); }
DI u32x2 pack4(f32x4 a) { return u32x2{cvtpk(a[0], a[1]), cvtpk(a[2], a[3])}; }
DI unsigned rfl(unsigned x) { return __builtin_amdgcn_readfirstlane(x); }
DI void glds16(const char* sbase, unsigned voff, unsigned lds_dst) {
    asm volatile("s_mov_b32 m0, %2\n\ts_nop 0\n\tglobal_load_lds_dwordx4 %0, %1" :: "v"(voff), "s"(sbase), "s"(lds_dst) : "memory");
}
template <int N> DI void wait_vm() { asm volatile("s_waitcnt vmcnt(%0)" :: "n"(N) : "memory"); }
DI bf16x8 lds_b128(lds_cptr p) { return *(const __attribute__((address_space(3))) bf16x8*)p; }
DI bf16x4 lds_b64(lds_cptr p) { return *(const volatile __attribute__((address_space(3))) bf16x4*)p; }
DI bf16x4 lds_tr(lds_cptr p) { return __builtin_bit_cast(bf16x4, __builtin_amdgcn_ds_read_tr16_b64_v4i16((__attribute__((address_space(3))) v4i16_t*)p)); }
DI void lds_st64(lds_ptr p, u32x2 v) { *(__attribute__((address_space(3))) u32x2*)p = v; }
DI bf16x8 cat8(bf16x4 a, bf16x4 b) { return __builtin_shufflevector(a, b, 0, 1, 2, 3, 4, 5, 6, 7); }
DI int swap23(int r) { return (r & ~12) | ((r & 4) << 1) | ((r & 8) >> 1); }
DI void touch_lines(const char* p, unsigned voff, unsigned lds_scratch) {
    asm volatile("s_mov_b32 m0, %2\n\ts_nop 0\n\tglobal_load_lds_dword %0, %1" :: "v"(voff), "s"(p), "s"(lds_scratch) : "memory");
}
DI void touch_addr(const char* p, unsigned lds_scratch) {
    asm volatile("s_mov_b32 m0, %1\n\ts_nop 0\n\tglobal_load_lds_dword %0, off" :: "v"(p), "s"(lds_scratch) : "memory");
}
#define CODE_ADDR(var, sym) asm volatile("s_getpc_b64 s[60:61]\n\ts_add_u32 s60, s60, " sym "@rel32@lo+4\n\ts_addc_u32 s61, s61, " sym "@rel32@hi+12\n\ts_mov_b64 %0, s[60:61]" : "=s"(var) :: "scc", "s60", "s61")
template <int POL> DI void st128(char* p, bf16x8 v) {
    if (POL == 0) *(bf16x8*)p = v;
    else if (POL == 1) asm volatile("global_store_dwordx4 %0, %1, off nt\n\ts_nop 1" :: "v"(p), "v"(v) : "memory");
    else if (POL == 2) asm volatile("global_store_dwordx4 %0, %1, off sc1\n\ts_nop 1" :: "v"(p), "v"(v) : "memory");
    else asm volatile("global_store_dwordx4 %0, %1, off sc0 sc1\n\ts_nop 1" :: "v"(p), "v"(v) : "memory");
}
#define POL1 2
#define POL2 0
#define POL3 2
#define MFMA16(a, b, c) __builtin_amdgcn_mfma_f32_16x16x32_bf16(a, b, c, 0, 0, 0)
#define BARRIER() do { asm volatile("" ::: "memory"); __builtin_amdgcn_s_waitcnt(0xC07F); __builtin_amdgcn_s_barrier(); asm volatile("" ::: "memory"); } while (0)
#define SBAR() __builtin_amdgcn_sched_barrier(0)
#define K1_RPM 1
template <int NM, int NR> DI void interleave() {
#pragma unroll
    for (int i = 0; i < NR / K1_RPM; ++i) { __builtin_amdgcn_sched_group_barrier(0x008, 1, 0); __builtin_amdgcn_sched_group_barrier(0x100, K1_RPM, 0); }
    __builtin_amdgcn_sched_group_barrier(0x008, NM - NR / K1_RPM, 0);
    SBAR();
}
#define STAMP_DECL
#define STAMP(kern, slot)
#define STAMP_DUMP(kern)
#define DIAG_DRAIN()

constexpr int A_PART = 6144, A_SLOT = 4 * A_PART, A_X = 3 * A_SLOT, A_LDS = A_X + 16 * 3072;
static_assert(4 * KT <= A_PART && 8 * VB <= A_PART && A_SLOT == 24576, "stage layout");

#define MFMA32(a, b, c) __builtin_amdgcn_mfma_f32_32x32x16_bf16(a, b, c, 0, 0, 0)
#define MFMA32R(a, b, c) __builtin_amdgcn_mfma_f32_32x32x8bf16_1k(a, b, c, 0, 0, 0)

DI void attn_exact(const char* kf, const char* vf, bf16x8 qm, bf16x4 qr, int lane, f32x16& o) {
    const int r32 = lane & 31, h = lane >> 5;
    float mrun = -3.0e38f;
#pragma unroll
    for (int r = 0; r < 16; ++r) o[r] = 0.f;
#pragma unroll 1
    for (int t = 0; t < 64; ++t) {
        const bf16x8 km = *(const bf16x8*)(kf + t * KT + lane * 16);
        const bf16x4 kr = *(const bf16x4*)(kf + t * KT + 1024 + r32 * 8);
        const int vo = r32 < 21 ? h * 336 + r32 * 16 : 672;
        const bf16x8 v0 = *(const bf16x8*)(vf + (2 * t) * VB + vo);
        const bf16x8 v1 = *(const bf16x8*)(vf + (2 * t + 1) * VB + vo);
        f32x16 s;
#pragma unroll
        for (int r = 0; r < 16; ++r) s[r] = 0.f;
        s = MFMA32(km, qm, s);
        s = MFMA32R(kr, qr, s);
        float mx = s[0];
#pragma unroll
        for (int r = 1; r < 16; ++r) mx = fmaxf(mx, s[r]);
        { auto rr = __builtin_amdgcn_permlane32_swap(__float_as_uint(mx), __float_as_uint(mx), false, false); mx = fmaxf(__uint_as_float(rr[0]), __uint_as_float(rr[1])); }
        const float mnew = fmaxf(mrun, mx), alpha = __builtin_amdgcn_exp2f(mrun - mnew);
        mrun = mnew;
        u32x4 p0, p1;
#pragma unroll
        for (int i = 0; i < 4; ++i) {
            p0[i] = cvtpk(__builtin_amdgcn_exp2f(s[2 * i] - mnew), __builtin_amdgcn_exp2f(s[2 * i + 1] - mnew));
            p1[i] = cvtpk(__builtin_amdgcn_exp2f(s[8 + 2 * i] - mnew), __builtin_amdgcn_exp2f(s[8 + 2 * i + 1] - mnew));
        }
#pragma unroll
        for (int r = 0; r < 16; ++r) o[r] *= alpha;
        o = MFMA32(v0, __builtin_bit_cast(bf16x8, p0), o);
        o = MFMA32(v1, __builtin_bit_cast(bf16x8, p1), o);
    }
}

DI float row_sum_of(const f32x16& o) {
    auto rr = __builtin_amdgcn_permlane32_swap(__float_as_uint(o[8]), __float_as_uint(o[8]), false, false);
    return __uint_as_float(rr[1]);
}

#define R_SA "v[64:79]"
#define R_SB "v[80:95]"
#define R_PL "v[100:103]"
#define R_PH "v[104:107]"
#define R_KM "v[108:111]"
#define R_KR "v[112:113]"
#define R_V0 "v[114:117]"
#define R_V1 "v[118:121]"
#define STR_(x) #x
#define STR(x) STR_(x)
#define MF16(d, a, b, c) "v_mfma_f32_32x32x16_bf16 " d ", " a ", " b ", " c "\n\t"
#define MF8(d, a, b, c) "v_mfma_f32_32x32x8_bf16 " d ", " a ", " b ", " c "\n\t"
#define CVT2(p0, p1) "v_cvt_pk_bf16_f32 v" STR(p0) ", v96, v97\n\tv_cvt_pk_bf16_f32 v" STR(p1) ", v98, v99\n\t"
#define EXP4(s0, s1, s2, s3) "v_exp_f32 v96, v" STR(s0) "\n\tv_exp_f32 v97, v" STR(s1) "\n\tv_exp_f32 v98, v" STR(s2) "\n\tv_exp_f32 v99, v" STR(s3) "\n\t"
#define GAP(p0, p1, s0, s1, s2, s3) CVT2(p0, p1) EXP4(s0, s1, s2, s3)
#define WAITL(n) "s_waitcnt lgkmcnt(" STR(n) ")\n\t"
#define RD128(d, a, off) "ds_read_b128 " d ", " a " offset:" STR(off) "\n\t"
#define RD64(d, a, off) "ds_read_b64 " d ", " a " offset:" STR(off) "\n\t"
#define TILE(MID, KOFF, V1OFF, V0OFF) \
    MF16(R_SB, R_KM, "%[qbm]", "0") RD128(R_KM, "v45", KOFF) GAP(106, 107, 64, 65, 66, 67) \
    MF8(R_SB, R_KR, "%[qbr]", R_SB) RD64(R_KR, "v46", KOFF) GAP(100, 101, 68, 69, 70, 71) \
    MF16("%[ob]", R_V1, R_PH, "%[ob]") RD128(R_V1, "v47", V1OFF) GAP(102, 103, 72, 73, 74, 75) \
    WAITL(3) MF16("%[oa]", R_V0, R_PL, "%[oa]") GAP(104, 105, 76, 77, 78, 79) \
    MID \
    WAITL(2) MF16(R_SA, R_KM, "%[qam]", "0") GAP(106, 107, 80, 81, 82, 83) \
    WAITL(1) MF8(R_SA, R_KR, "%[qar]", R_SA) GAP(100, 101, 84, 85, 86, 87) \
    WAITL(0) MF16("%[oa]", R_V1, R_PH, "%[oa]") GAP(102, 103, 88, 89, 90, 91) \
    MF16("%[ob]", R_V0, R_PL, "%[ob]") RD128(R_V0, "v47", V0OFF) GAP(104, 105, 92, 93, 94, 95)
#define TILE_LAST(MID, PRE_K, V1OFF, PRE_V0) \
    MF16(R_SB, R_KM, "%[qbm]", "0") PRE_K RD128(R_KM, "v45", 0) GAP(106, 107, 64, 65, 66, 67) \
    MF8(R_SB, R_KR, "%[qbr]", R_SB) RD64(R_KR, "v46", 0) GAP(100, 101, 68, 69, 70, 71) \
    MF16("%[ob]", R_V1, R_PH, "%[ob]") RD128(R_V1, "v47", V1OFF) GAP(102, 103, 72, 73, 74, 75) \
    WAITL(3) MF16("%[oa]", R_V0, R_PL, "%[oa]") GAP(104, 105, 76, 77, 78, 79) \
    MID \
    WAITL(2) MF16(R_SA, R_KM, "%[qam]", "0") GAP(106, 107, 80, 81, 82, 83) \
    WAITL(1) MF8(R_SA, R_KR, "%[qar]", R_SA) GAP(100, 101, 84, 85, 86, 87) \
    WAITL(0) MF16("%[oa]", R_V1, R_PH, "%[oa]") GAP(102, 103, 88, 89, 90, 91) \
    MF16("%[ob]", R_V0, R_PL, "%[ob]") PRE_V0 RD128(R_V0, "v47", 0) GAP(104, 105, 92, 93, 94, 95)

#define P0 2
#define P1 2
#define P2 1
#define P3 1
#define P4 0
#define P5 0
#define P6 3
#define P7 3
#define PRIO(n) "s_setprio " STR(n) "\n\t"
#define ASM_STAMP(r)
#define TOUCH_PF "global_load_lds_dword v42, %[pf]\n\t"
#define TOUCH_PO "global_load_lds_dword v41, %[po]\n\t"
#define DMA2(SLOTREG) \
    "s_cmp_ge_u32 %[wid], 8\n\ts_cbranch_scc1 5f\n\t" \
    "s_lshr_b32 s46, s45, 1\n\ts_add_u32 s46, s46, %[blk]\n\ts_and_b32 s46, s46, 3\n\ts_lshl_b32 s46, s46, 2\n\t" \
    "s_and_b32 s47, s45, 1\n\ts_add_u32 s46, s46, s47\n\ts_lshl_b32 s46, s46, 2\n\ts_add_u32 s45, s45, 1\n\t" \
    "s_mul_i32 s47, s46, 1296\n\ts_add_u32 s60, s48, s47\n\ts_addc_u32 s61, s49, 0\n\t" \
    "s_add_u32 m0, %[d0], " SLOTREG "\n\ts_mul_i32 s47, s46, %[p1]\n\tglobal_load_lds_dwordx4 v40, s[60:61]\n\t" \
    "s_add_u32 s60, s50, s47\n\ts_addc_u32 s61, s51, 0\n\t" \
    "s_add_u32 m0, m0, 0x2000\n\ts_mul_i32 s47, s46, 1376\n\tglobal_load_lds_dwordx4 v40, s[60:61]\n\t" \
    "s_add_u32 s60, s54, s47\n\ts_addc_u32 s61, s55, 0\n\t" \
    "s_add_u32 m0, m0, 0x2000\n\ts_nop 0\n\tglobal_load_lds_dwordx4 v40, s[60:61]\n\t" \
    "5:\n\t"

extern "C" __global__ __launch_bounds__(1024, 4) void attn_kernel(char* ws, const char* woA, const char* woB) {
    extern __shared__ __attribute__((aligned(16))) char smem[];
    const int tid = threadIdx.x, lane = tid & 63, wid = (int)rfl(tid >> 6);
    const int xcd = blockIdx.x & 7, idx = blockIdx.x >> 3;
    const int qblk = 2 * (xcd & 1) + (idx & 1), branch = idx >> 4, head = 8 * (xcd >> 1) + ((idx >> 1) & 7), kblk = 2 * (xcd & 1);
    const int kvh = branch ? 32 : head;
    const int qg = wid & 7, kh = wid >> 3;
    const unsigned kf_off = KF_OFF + kvh * KF_HEAD, vf_off = VF_OFF + kvh * VF_HEAD;
    const unsigned lds0 = (unsigned)(uintptr_t)smem;
    STAMP_DECL;
    STAMP(2, 0);
    const unsigned w8 = wid & 7, w1k = w8 * 1024;
    const unsigned src0 = kf_off + w1k + (w8 >= 6 ? 8 * KT - 6144 : 0);
    const unsigned src1 = w8 < 4 ? kf_off + 8 * KT + 2048 + w1k : vf_off + w1k - 4096, pitch1 = w8 < 4 ? KT : 2 * VB;
    const unsigned src2 = vf_off + w1k + (w8 < 2 ? 4096 : 16 * VB - 2048);
    const unsigned d0 = lds0 + wid * 1024;
    if (wid < 8) {
        const unsigned t0 = kblk * 16, l16 = lane * 16;
        glds16(ws + (src0 + t0 * KT), l16, d0); glds16(ws + (src1 + t0 * pitch1), l16, d0 + 8192); glds16(ws + (src2 + t0 * 2 * VB), l16, d0 + 16384);
    }
    __builtin_amdgcn_sched_barrier(0);
    const int qrow0 = qblk * 512 + qg * 64;
    const unsigned qoff = rfl(QF_OFF + (unsigned)(branch * 32 + head) * HEAD40 + (unsigned)(qrow0 + 32 * kh) * 40), qoffb = rfl(QF_OFF + (unsigned)(branch * 32 + head) * HEAD40 + (unsigned)(qrow0 + 32 - 32 * kh) * 40);
    const unsigned kbase = rfl(lds0 + kh * A_PART), vbase = rfl(lds0 + 2 * A_PART + kh * A_PART);
    const char* pf; unsigned pfmax;
    { const char* code; CODE_ADDR(code, "outproj_kernel");
      pf = wid < 4 ? (xcd >> 2 ? woB : woA) + (idx * 200 + wid * 50) * 128 : code + (wid == 5 ? 8192 : 0); pfmax = wid < 4 ? 49 : 63; }
    const char* po = ws + OF_OFF + (size_t)((unsigned)(branch * 32 + head) * HEAD40 + (unsigned)(qrow0 + 32 * kh) * 40);
    bf16x8 qam, qbm; bf16x4 qar, qbr;
    f32x16 oa, ob;
    asm volatile(
        ASM_STAMP("s[72:73]")
        "v_lshlrev_b32 v40, 4, %[lane]\n\t"
        "s_mov_b64 s[52:53], %[ws]\n\t"
        "s_add_u32 s48, s52, %[src0]\n\ts_addc_u32 s49, s53, 0\n\ts_add_u32 s50, s52, %[src1]\n\ts_addc_u32 s51, s53, 0\n\ts_add_u32 s54, s52, %[src2]\n\ts_addc_u32 s55, s53, 0\n\t"
        "s_mov_b32 s45, 1\n\t"
        "v_and_b32 v41, 31, %[lane]\n\tv_lshrrev_b32 v42, 5, %[lane]\n\t"
        "s_add_u32 s62, s52, %[qoff]\n\ts_addc_u32 s63, s53, 0\n\ts_add_u32 s56, s52, %[qoffb]\n\ts_addc_u32 s57, s53, 0\n\t"
        "v_lshlrev_b32 v43, 3, v41\n\t"
        "v_mov_b64 %[qar], 0\n\tv_mov_b64 %[qbr], 0\n\t"
        "global_load_dwordx4 %[qam], v40, s[62:63]\n\tglobal_load_dwordx4 %[qbm], v40, s[56:57]\n\t"
        "s_mov_b32 exec_hi, 0\n\t"
        "global_load_dwordx2 %[qar], v43, s[62:63] offset:1024\n\tglobal_load_dwordx2 %[qbr], v43, s[56:57] offset:1024\n\t"
        "s_mov_b32 exec_hi, -1\n\t"
        "v_cmp_eq_u32 vcc, 0, v42\n\t"
        "v_add_u32 v45, %[kbase], v40\n\t"
        "v_add_u32 v46, 0x400, v43\n\tv_mov_b32 v48, 0x500\n\ts_nop 0\n\t"
        "v_cndmask_b32 v46, v48, v46, vcc\n\tv_add_u32 v46, %[kbase], v46\n\t"
        "v_cmp_gt_u32 vcc, 21, v41\n\t"
        "v_mul_u32_u24 v47, 0x150, v42\n\tv_lshl_add_u32 v47, v41, 4, v47\n\tv_mov_b32 v48, 0x2a0\n\t"
        "v_cndmask_b32 v47, v48, v47, vcc\n\tv_add_u32 v47, %[vbase], v47\n\t"
        "v_mov_b32 v118, 0\n\tv_mov_b32 v119, 0\n\tv_mov_b32 v120, 0\n\tv_mov_b32 v121, 0\n\t"
        "v_mov_b32 v96, 0\n\tv_mov_b32 v97, 0\n\tv_mov_b32 v98, 0\n\tv_mov_b32 v99, 0\n\t"
        "v_mov_b32 v104, 0\n\tv_mov_b32 v105, 0\n\t"
        "v_mfma_f32_32x32x16_bf16 %[oa], v[118:121], v[118:121], 0\n\tv_mfma_f32_32x32x16_bf16 %[ob], v[118:121], v[118:121], 0\n\t"
        "s_mov_b32 s40, 0\n\t"
        "s_mov_b32 s41, 0\n\t"
        "s_mov_b32 s44, 24576\n\t"
        "s_waitcnt vmcnt(4)\n\t" ASM_STAMP("s[66:67]") "s_barrier\n\t" ASM_STAMP("s[68:69]")
        RD128(R_KM, "v45", 0) RD64(R_KR, "v46", 0) RD128(R_V0, "v47", 0)
        "s_waitcnt vmcnt(0)\n\t"
        DMA2("s44")
        WAITL(0)
        MF16(R_SA, R_KM, "%[qam]", "0") MF8(R_SA, R_KR, "%[qar]", R_SA)
        "s_nop 15\n\ts_nop 3\n\t"
        ASM_STAMP("s[70:71]")
        "2:\n\t"
        "s_add_u32 s42, s41, 24576\n\ts_cmp_eq_u32 s42, 73728\n\ts_cselect_b32 s42, 0, s42\n\t"
        "s_sub_u32 s43, s42, s41\n\t"
        "s_add_u32 s44, s42, 24576\n\ts_cmp_eq_u32 s44, 73728\n\ts_cselect_b32 s44, 0, s44\n\t"
        PRIO(P0) TILE(PRIO(P1), 1296, 688, 1376)
        PRIO(P2) TILE(PRIO(P3), 2592, 2064, 2752)
        PRIO(P4) TILE(PRIO(P5), 3888, 3440, 4128)
        PRIO(P6) TILE_LAST(PRIO(P7),
            "s_waitcnt vmcnt(0)\n\ts_barrier\n\t"
            "v_add_u32 v45, s43, v45\n\tv_add_u32 v46, s43, v46\n\t"
            "s_cmp_ge_u32 s40, 6\n\ts_cbranch_scc1 3f\n\t"
            DMA2("s44")
            "s_branch 4f\n\t"
            "3:\n\t"
            "s_cmp_lg_u32 s40, 6\n\ts_cbranch_scc1 4f\n\t"
            "v_lshrrev_b32 v41, 4, v40\n\tv_min_u32 v42, %[pfmax], v41\n\tv_min_u32 v41, 9, v41\n\tv_lshlrev_b32 v42, 7, v42\n\tv_lshlrev_b32 v41, 7, v41\n\t"
            "s_add_u32 m0, %[d0], s44\n\ts_nop 0\n\t"
            TOUCH_PF TOUCH_PO
            "4:\n\t",
            4816, "v_add_u32 v47, s43, v47\n\t")
        "s_mov_b32 s41, s42\n\t"
        "s_add_u32 s40, s40, 1\n\t"
        "s_cmp_lt_u32 s40, 8\n\ts_cbranch_scc1 2b\n\t"
        "s_nop 1\n\t" CVT2(106, 107) "s_nop 1\n\t"
        MF16("%[ob]", R_V1, R_PH, "%[ob]")
        "s_waitcnt lgkmcnt(0)\n\ts_nop 15\n\ts_nop 3\n\t"
        : [oa] "=&v"(oa), [ob] "=&v"(ob), [qam] "=&v"(qam), [qbm] "=&v"(qbm), [qar] "=&v"(qar), [qbr] "=&v"(qbr)
        : [lane] "v"(lane), [ws] "s"(ws), [src0] "s"(src0), [src1] "s"(src1), [src2] "s"(src2), [p1] "s"(pitch1), [blk] "s"(kblk), [qoff] "s"(qoff), [qoffb] "s"(qoffb),
          [kbase] "s"(kbase), [vbase] "s"(vbase), [d0] "s"(d0), [wid] "s"(wid), [pf] "s"(pf), [po] "s"(po), [pfmax] "s"(pfmax)
        : "memory", "scc", "vcc", "s40", "s41", "s42", "s43", "s44", "s45", "s46", "s47", "s48", "s49", "s50", "s51", "s52", "s53", "s54", "s55", "s56", "s57", "s60", "s61", "s62", "s63",
          "s64", "s65", "s66", "s67", "s68", "s69", "s70", "s71", "s72", "s73",
          "v40", "v41", "v42", "v43", "v44", "v45", "v46", "v47", "v48",
          "v64", "v65", "v66", "v67", "v68", "v69", "v70", "v71", "v72", "v73", "v74", "v75", "v76", "v77", "v78", "v79",
          "v80", "v81", "v82", "v83", "v84", "v85", "v86", "v87", "v88", "v89", "v90", "v91", "v92", "v93", "v94", "v95",
          "v96", "v97", "v98", "v99", "v100", "v101", "v102", "v103", "v104", "v105", "v106", "v107", "v108", "v109", "v110", "v111",
          "v112", "v113", "v114", "v115", "v116", "v117", "v118", "v119", "v120", "v121");
    STAMP(2, 2);
    const int r32 = lane & 31, h = lane >> 5;
    {
        lds_ptr xb = (lds_ptr)smem + A_X + wid * 3072 + lane * 16;
#pragma unroll
        for (int i = 0; i < 3; ++i) *(__attribute__((address_space(3))) f32x4*)(xb + i * 1024) = f32x4{ob[4 * i], ob[4 * i + 1], ob[4 * i + 2], ob[4 * i + 3]};
    }
    f32x16 o = oa;
    BARRIER();
    {
        lds_cptr xb = (lds_cptr)smem + A_X + (wid ^ 8) * 3072 + lane * 16;
#pragma unroll
        for (int i = 0; i < 3; ++i) { const f32x4 v = *(const __attribute__((address_space(3))) f32x4*)(xb + i * 1024);
#pragma unroll
            for (int e = 0; e < 4; ++e) o[4 * i + e] += v[e]; }
    }
    float l = row_sum_of(o);
    const float mag = ((fabsf(o[0]) + fabsf(o[1])) + (fabsf(o[2]) + fabsf(o[3]))) + ((fabsf(o[4]) + fabsf(o[5])) + (fabsf(o[6]) + fabsf(o[7]))) + ((fabsf(o[8]) + fabsf(o[9])) + (fabsf(o[10]) + fabsf(o[11])));
    const bool ok = l >= 0x1p-60f && l <= 0x1p60f && mag <= 3.0e38f;
    if (__builtin_expect(!__all(ok), 0)) {
        attn_exact(ws + kf_off, ws + vf_off, qam, qar, lane, o);
        l = row_sum_of(o);
    }
    const float inv = __builtin_amdgcn_rcpf(l);
    {
        lds_ptr so = (lds_ptr)smem + A_X + (wid ^ 8) * 3072;
        lds_st64(so + r32 * 40 + 8 * h, u32x2{cvtpk(o[0] * inv, o[1] * inv), cvtpk(o[2] * inv, o[3] * inv)});
        lds_st64(so + r32 * 40 + 16 + 8 * h, u32x2{cvtpk(o[4] * inv, o[5] * inv), cvtpk(o[6] * inv, o[7] * inv)});
        if (h == 0) lds_st64(so + r32 * 40 + 32, u32x2{cvtpk(o[8] * inv, o[9] * inv), cvtpk(o[10] * inv, o[11] * inv)});
        asm volatile("" ::: "memory"); __builtin_amdgcn_s_waitcnt(0xC07F); asm volatile("" ::: "memory");
        char* od = ws + OF_OFF + (size_t)((unsigned)(branch * 32 + head) * HEAD40 + (unsigned)(qrow0 + 32 * kh) * 40) + lane * 16;
        st128<POL2>(od, lds_b128((lds_cptr)so + lane * 16));
        if (lane < 16) st128<POL2>(od + 1024, lds_b128((lds_cptr)so + 1024 + lane * 16));
    }
    DIAG_DRAIN();
    STAMP(2, 7);
    STAMP_DUMP(2);
}

#define K3_RPM 2
constexpr int K3_X = 0, K3_W = 32 * 3840, K3_LDS = K3_W + 4 * 8192;
constexpr int k3_total(int t) { int n = 14; for (int u = 0; u < t; ++u) n += (u <= 16 ? 2 : 0) + (u <= 10 ? 2 : 0); return n; }
constexpr int k3_allowed(int t) {
    int last = t <= 2 ? 10 + 2 * t : k3_total(t - 3) + 2;
    const int xg = t == 5 ? 3 : t == 10 ? 7 : t == 15 ? 10 : -1;
    if (xg >= 0) { const int e = k3_total(xg) + 4; if (e > last) last = e; }
    return k3_total(t) - last;
}
DI int k3_krow(int c, int e) { const int P = c / 5, r = c - 5 * P; return r < 4 ? (2 * P + (r >> 1)) * 20 + (r & 1) * 8 + e : (2 * P + (e >> 2)) * 20 + 16 + (e & 3); }

DI unsigned k3_xoff(int o, int rot) { const int hd = o / 3840; return (unsigned)(((hd + 8 * rot) & 31) * HEAD40 + (o - hd * 3840)); }
template <int T> DI void k3_load_steps(const char* xsrc, const char* wsrc, const unsigned (&wo)[5][2], int lw, int lane, unsigned lds0, int rot) {
    if constexpr (T < 20) {
        wait_vm<k3_allowed(T)>();
        asm volatile("s_barrier" ::: "memory");
        if constexpr (T + 3 < 20) {
            constexpr int t = T + 3;
#pragma unroll
            for (int i = 0; i < 2; ++i) glds16(wsrc + (size_t)((t / 5 + rot) & 3) * 160 * (DM * 2), wo[t % 5][i], lds0 + K3_W + (t & 3) * 8192 + (2 * lw + i) * 1024);
        }
        if constexpr (T <= 10) {
#pragma unroll
            for (int i = 0; i < 2; ++i) { const int pc = lw + 4 * (8 + 2 * T + i); glds16(xsrc, k3_xoff(pc * 1024 + lane * 16, rot), lds0 + K3_X + pc * 1024); }
        }
        k3_load_steps<T + 1>(xsrc, wsrc, wo, lw, lane, lds0, rot);
    }
}

extern "C" __global__ __launch_bounds__(512, 2) void outproj_kernel(char* ws, const char* woA, const char* woB, char* out) {
    extern __shared__ __attribute__((aligned(16))) char smem[];
    const int tid = threadIdx.x, lane = tid & 63, wid = (int)rfl(tid >> 6);
    const int xcd = blockIdx.x & 7, idx = blockIdx.x >> 3, br = xcd >> 2;
    const int blk = 2 * (xcd & 1) + ((xcd >> 1) & 1), rot = xcd >> 1;
    if (idx >= 30) return;
    STAMP_DECL;
    const int rl = idx / 5, ct = idx - 5 * rl, row0 = blk * 512 + (rl < 5 ? rl * 96 : 416);
    if (wid >= 4) {
        const int lw = wid - 4;
        const char* xsrc = ws + OF_OFF + (size_t)br * 32 * HEAD40 + row0 * 40;
        const char* wsrc = (br ? woB : woA) + ct * 256;
        const unsigned lds0 = (unsigned)(uintptr_t)smem;
#pragma unroll
        for (int j = 0; j < 8; ++j) { const int pc = lw + 4 * j; glds16(xsrc, k3_xoff(pc * 1024 + lane * 16, rot), lds0 + K3_X + pc * 1024); }
        SBAR();
        unsigned wo[5][2];
#pragma unroll
        for (int tt = 0; tt < 5; ++tt)
#pragma unroll
            for (int i = 0; i < 2; ++i) {
                const int rho = 4 * (2 * lw + i) + (lane >> 4), s16 = lane & 15, kap = swap23(rho);
                wo[tt][i] = (unsigned)(k3_krow(4 * tt + (kap >> 3), kap & 7) * (DM * 2) + (((s16 >> 1) ^ (rho & 7)) << 5) + (s16 & 1) * 16);
            }
#pragma unroll
        for (int t = 0; t < 3; ++t)
#pragma unroll
            for (int i = 0; i < 2; ++i) glds16(wsrc + (size_t)rot * 160 * (DM * 2), wo[t][i], lds0 + K3_W + t * 8192 + (2 * lw + i) * 1024);
        k3_load_steps<0>(xsrc, wsrc, wo, lw, lane, lds0, rot);
        asm volatile("s_barrier\n\ts_barrier" ::: "memory");
    } else {
        lds_cptr lds = (lds_cptr)smem;
        const int wm = wid >> 1, wn = wid & 1, m = lane & 15, g = lane >> 4, q = (lane & 15) >> 2, p = lane & 3;
        unsigned xlo[5], xhi[5], wa[4];
#pragma unroll
        for (int tt = 0; tt < 5; ++tt) {
            const int c = 4 * tt + g, P = c / 5, r = c - 5 * P, rb = (48 * wm + m) * 40;
            xlo[tt] = (unsigned)(K3_X + rb + (r < 4 ? (2 * P + (r >> 1)) * 3840 + (r & 1) * 16 : 2 * P * 3840 + 32));
            xhi[tt] = (unsigned)(K3_X + rb + (r < 4 ? (2 * P + (r >> 1)) * 3840 + (r & 1) * 16 + 8 : (2 * P + 1) * 3840 + 32));
        }
        { const int sx = (g & 1) * 4 + q;
#pragma unroll
          for (int j = 0; j < 4; ++j) wa[j] = (unsigned)(K3_W + ((g >> 1) * 16 + sx) * 256 + (((4 * wn + j) ^ sx) << 5) + p * 8); }
        f32x4 acc[3][4];
#pragma unroll
        for (int i = 0; i < 3; ++i)
#pragma unroll
            for (int j = 0; j < 4; ++j) acc[i][j] = f32x4{0.f, 0.f, 0.f, 0.f};
        bf16x8 xf[2][3], wf[2][4];
        auto load = [&](int t) {
            const int tt = t % 5, u = t / 5;
#pragma unroll
            for (int i = 0; i < 3; ++i) xf[t & 1][i] = cat8(lds_b64(lds + xlo[tt] + u * 30720 + i * 640), lds_b64(lds + xhi[tt] + u * 30720 + i * 640));
#pragma unroll
            for (int j = 0; j < 4; ++j) wf[t & 1][j] = cat8(lds_tr(lds + wa[j] + (t & 3) * 8192), lds_tr(lds + wa[j] + (t & 3) * 8192 + 2048));
        };
        STAMP(3, 0);
        BARRIER();
        STAMP(3, 1);
        load(0);
        SBAR();
#pragma unroll
        for (int t = 0; t < 20; ++t) {
            if (t + 1 < 20) { BARRIER(); load(t + 1); }
#pragma unroll
            for (int i = 0; i < 3; ++i)
#pragma unroll
                for (int j = 0; j < 4; ++j) acc[i][j] = MFMA16(wf[t & 1][j], xf[t & 1][i], acc[i][j]);
            if (t + 1 < 20) {
#pragma unroll
                for (int i = 0; i < 14 / K3_RPM; ++i) { __builtin_amdgcn_sched_group_barrier(0x100, K3_RPM, 0); __builtin_amdgcn_sched_group_barrier(0x008, 1, 0); }
                __builtin_amdgcn_sched_group_barrier(0x100, 14, 0); __builtin_amdgcn_sched_group_barrier(0x008, 12, 0);
            }
            SBAR();
        }
        STAMP(3, 2);
        BARRIER();
        STAMP(3, 3);
        lds_ptr st = (lds_ptr)smem;
#pragma unroll
        for (int i = 0; i < 3; ++i) {
            const int row = 48 * wm + 16 * i + m;
#pragma unroll
            for (int j = 0; j < 4; ++j) { const int b = (64 * wn + 16 * j + 4 * g) * 2; lds_st64(st + row * 256 + ((((b >> 4) ^ (row & 15)) << 4) | (b & 8)), pack4(acc[i][j])); }
        }
        BARRIER();
        STAMP(3, 4);
    }
#pragma unroll
    for (int it = 0; it < 3; ++it) {
        const int o = it * 8192 + tid * 16, row = o >> 8, s = (o >> 4) & 15;
        st128<POL3>(out + (size_t)(row0 + row) * 2560 + br * 1280 + ct * 256 + ((s ^ (row & 15)) << 4), lds_b128((lds_cptr)smem + o));
    }
    if (wid < 4) { STAMP(3, 5); DIAG_DRAIN(); STAMP(3, 7); STAMP_DUMP(3); }
}

constexpr int NSTAGE = DM / 64;
constexpr int K1_X = 0, K1_W = 16384, K1_W2 = K1_W + 24576, K1_SLOT = K1_W2 + 3072, K1_LDS = 3 * K1_SLOT + 256;

template <int NP> DI void k1_loader(const char* xg, const char* wg, const char* w2g, int wpitch, int lw, int lane, unsigned lds0, const char* tdst, unsigned dstride) {
    unsigned xo[4], wo[6], w2o = 0;
#pragma unroll
    for (int i = 0; i < 4; ++i) { const int row = (4 * lw + i) * 8 + (lane >> 3), pos = lane & 7; xo[i] = (unsigned)(row * (DM * 2) + ((pos ^ ((row >> 1) & 7)) << 4)); }
#pragma unroll
    for (int j = 0; j < 3; ++j) {
        const int o = j * 1024 + lane * 16, r = (o * 171) >> 16, b = o - r * 384;
        wo[j] = (unsigned)((16 * lw + 8 * (r >> 2) + (r & 3)) * wpitch + (((b >> 5) ^ ((r >> 1) & 3)) << 5) + (b & 16));
        wo[j + 3] = wo[j] + 4u * (unsigned)wpitch;
    }
    const int p2 = lw;
    if (NP == 11) { const int o = p2 * 1024 + lane * 16; w2o = (unsigned)(o < 2560 ? o : 2544); }
    auto issue_w = [&](int s, int slot) {
        const unsigned sl = lds0 + (unsigned)slot * K1_SLOT;
        const char* wsrc = wg + (size_t)s * 64 * wpitch;
#pragma unroll
        for (int i = 0; i < 6; ++i) glds16(wsrc, wo[i], sl + K1_W + (6 * lw + i) * 1024);
        if (NP == 11) glds16(w2g + (size_t)s * 2560, w2o, sl + K1_W2 + p2 * 1024);
    };
    auto issue = [&](int s, int slot) {
        const unsigned sl = lds0 + (unsigned)slot * K1_SLOT;
        const char* xs = xg + (size_t)s * 128;
#pragma unroll
        for (int i = 0; i < 4; ++i) glds16(xs, xo[i], sl + K1_X + (4 * lw + i) * 1024);
        issue_w(s, slot);
    };
    issue_w(0, 0); issue(1, 1);
    int nslot = 2;
#pragma unroll 1
    for (int s = 0; s < NSTAGE; ++s) {
        if (s < NSTAGE - 1) wait_vm<NP>(); else wait_vm<0>();
        asm volatile("s_barrier" ::: "memory");
        if (s + 2 < NSTAGE) { issue(s + 2, nslot); nslot = nslot == 2 ? 0 : nslot + 1; }
    }
}

#define TOUCH_ALL 0
struct K1Frag { bf16x8 x[4]; bf16x8 w[5]; bf16x8 w2; };

#define STP_PARAM
#define STP_ARG
template <int KIND, int XTRA> DI void k1_consumer(char* ws, char* smem, int wid, int lane, int shift32, int xo, int row0, const char* ahead STP_PARAM) {
    lds_cptr lds = (lds_cptr)smem;
    const int wm = wid >> 1, wn = wid & 1, m = lane & 15, g = lane >> 4, q = (lane & 15) >> 2, p = lane & 3;
    const unsigned xa0 = K1_X + (unsigned)((64 * wm + m) * 128 + ((g ^ ((m >> 1) & 7)) << 4));
    unsigned wa[5];
    { const int gx = ((g & 1) << 1) | (q >> 1);
#pragma unroll
      for (int j = 0; j < 5; ++j) wa[j] = K1_W + (unsigned)(((g >> 1) * 16 + (g & 1) * 4 + q) * 384 + (((shift32 + 5 * wn + j) ^ gx) << 5) + p * 8); }
    const unsigned w2a = K1_W2 + (unsigned)((8 * g + q) * 40 + p * 8 + xo * 2);
    f32x4 acc[4][5], acc2[2];
#pragma unroll
    for (int i = 0; i < 4; ++i)
#pragma unroll
        for (int j = 0; j < 5; ++j) acc[i][j] = f32x4{0.f, 0.f, 0.f, 0.f};
    acc2[0] = acc2[1] = f32x4{0.f, 0.f, 0.f, 0.f};
    auto load = [&](K1Frag& f, unsigned sl, int kk) {
        lds_cptr b = lds + sl;
#pragma unroll
        for (int i = 0; i < 4; ++i) f.x[i] = lds_b128(b + ((xa0 ^ (kk * 64)) + i * 2048));
#pragma unroll
        for (int j = 0; j < 5; ++j) f.w[j] = cat8(lds_tr(b + wa[j] + kk * 12288), lds_tr(b + wa[j] + kk * 12288 + 3072));
        if (XTRA) f.w2 = cat8(lds_tr(b + w2a + kk * 1280), lds_tr(b + w2a + kk * 1280 + 160));
    };
    auto mma = [&](const K1Frag& f) {
#pragma unroll
        for (int i = 0; i < 4; ++i)
#pragma unroll
            for (int j = 0; j < 5; ++j) acc[i][j] = KIND == 2 ? MFMA16(f.x[i], f.w[j], acc[i][j]) : MFMA16(f.w[j], f.x[i], acc[i][j]);
        if (XTRA) {
#pragma unroll
            for (int i = 0; i < 2; ++i) { const bf16x8 xx = wn ? f.x[2 + i] : f.x[i]; acc2[i] = XTRA == 2 ? MFMA16(xx, f.w2, acc2[i]) : MFMA16(f.w2, xx, acc2[i]); }
        }
    };
    constexpr int CH = KIND == 0 ? 5120 : KIND == 1 ? 4 * KT : 8 * VB, TP = KIND == 0 ? 1280 : KT;
    unsigned sa[5], sh[5];
#pragma unroll
    for (int j = 0; j < 5; ++j) {
        const int col = 80 * wn + 16 * j + (KIND == 2 ? m : 4 * g), hl = (col * 205) >> 12, d = col - hl * 20;
        if (KIND == 2) { sa[j] = (unsigned)(hl * CH + 4 * wm * VB + (g & 1) * 336 + (g >> 1) * 8 + d * 16); sh[j] = 0; }
        else { const bool rem = d == 16; sa[j] = (unsigned)(hl * CH + 2 * wm * TP + (rem ? 1024 + m * 8 : (d >> 3) * 512 + m * 16 + (d & 4) * 2)); sh[j] = sa[j] + (rem ? 128 : 256); asm volatile("" : "+v"(sh[j])); }
        asm volatile("" : "+v"(sa[j]));
    }
    K1Frag f0, f1;
    STAMP(1, 0);
    BARRIER();
    STAMP(1, 1);
    const unsigned scratch = rfl((unsigned)(uintptr_t)smem + 3 * K1_SLOT);
    load(f0, 0, 0);
    int slot = 0;
    constexpr int NM = XTRA ? 22 : 20, NR = XTRA ? 16 : 14;
    SBAR();
#pragma unroll 1
    for (int s = 0; s < NSTAGE - 1; ++s) {
        load(f1, (unsigned)slot * K1_SLOT, 1);
        mma(f0);
        interleave<NM, NR>();
        slot = slot == 2 ? 0 : slot + 1;
        BARRIER();
        if (wid == (s & 3)) {
            if (s == 8) { const char* code; CODE_ADDR(code, "attn_kernel"); touch_lines(code, lane * 128, scratch); }
            SBAR();
        }
        load(f0, (unsigned)slot * K1_SLOT, 0);
        mma(f1);
        interleave<NM, NR>();
    }
    load(f1, (unsigned)slot * K1_SLOT, 1);
    mma(f0);
    interleave<NM, NR>();
    mma(f1);
    STAMP(1, 2);
    BARRIER();
    STAMP(1, 3);
    lds_ptr st = (lds_ptr)smem;
#pragma unroll
    for (int j = 0; j < 5; ++j) {
        if (KIND == 2) {
#pragma unroll
            for (int i = 0; i < 4; ++i) lds_st64(st + sa[j] + i * VB, pack4(acc[i][j]));
        } else {
            const float sc = KIND == 0 ? QSCALE : 1.0f;
            lds_st64(st + sa[j], pack4(acc[0][j] * sc)); lds_st64(st + sa[j] + TP, pack4(acc[2][j] * sc));
            lds_st64(st + sh[j], pack4(acc[1][j] * sc)); lds_st64(st + sh[j] + TP, pack4(acc[3][j] * sc));
        }
    }
    if (XTRA == 1) {
        const int d0 = xo + 4 * g;
        if (d0 < (xo ? 20 : 12)) {
#pragma unroll
            for (int i = 0; i < 2; ++i) {
                const int row = row0 + 64 * wm + 32 * wn + 16 * i + m, key = row & 31;
                *(u32x2*)(ws + KF_OFF + (size_t)32 * KF_HEAD + (row >> 5) * KT + (d0 < 16 ? (d0 >> 3) * 512 + key * 16 + (d0 & 4) * 2 : 1024 + key * 8)) = pack4(acc2[i]);
            }
        }
        if (xo == 0 && lane == 0 && wn == 0) {
#pragma unroll
            for (int i = 0; i < 2; ++i) *(u32x4*)(ws + KF_OFF + (size_t)32 * KF_HEAD + ((row0 >> 5) + 2 * wm + i) * KT + 1280) = u32x4{0u, 0u, 0u, 0u};
        }
    }
    if (XTRA == 2) {
        const int d = xo + m;
#pragma unroll
        for (int i = 0; i < 2; ++i) {
            char* dst = ws + VF_OFF + (size_t)32 * VF_HEAD + ((row0 >> 4) + 4 * wm + 2 * wn + i) * VB;
            if (d < (xo ? 20 : 12)) *(u32x2*)(dst + (g & 1) * 336 + (g >> 1) * 8 + d * 16) = pack4(acc2[i]);
            if (xo == 0 && m == 15) *(u32x2*)(dst + (g & 1) * 336 + (g >> 1) * 8 + 320) = u32x2{0x3F803F80u, 0x3F803F80u};
            if (xo == 0 && lane == 14) *(u32x4*)(dst + 672) = u32x4{0u, 0u, 0u, 0u};
        }
    }
    BARRIER();
    STAMP(1, 4);
}

template <int CH> DI void k1_copy_out(const char* smem, char* dst, unsigned stride, int wid, int lane) {
    lds_cptr src = (lds_cptr)smem + wid * CH + lane * 16;
    char* d = dst + (size_t)wid * stride + lane * 16;
    bf16x8 v[5];
#pragma unroll
    for (int i = 0; i < 5; ++i) v[i] = lds_b128(src + i * 1024);
#pragma unroll
    for (int i = 0; i < 5; ++i) st128<POL1>(d + i * 1024, v[i]);
    if (CH > 5120 && lane < (CH - 5120) / 16) st128<POL1>(d + 5120, lds_b128(src + 5120));
}

extern "C" __global__ __launch_bounds__(512, 2) void proj_kernel(const char* xA, const char* wqkv, const char* xB, const char* wq, const char* wk, const char* wv, char* ws) {
    extern __shared__ __attribute__((aligned(16))) char smem[];
    const int tid = threadIdx.x, lane = tid & 63, wid = (int)rfl(tid >> 6);
    {
        const char* code; CODE_ADDR(code, "proj_kernel");
        if (wid < 3) touch_lines(code + wid * 8192, (wid == 2 ? (lane < 24 ? lane : 23) : lane) * 128, rfl((unsigned)(uintptr_t)smem + 3 * K1_SLOT));
        __builtin_amdgcn_sched_barrier(0);
    }
#define MAP1 0
    const int xcd = blockIdx.x & 7, idx = blockIdx.x >> 3, blk = xcd & 3, hh = xcd >> 2, grp = idx >> 3;
    const bool isB = grp == 1;
    const int rt = 4 * blk + (idx & 3);
    const int ct = (grp < 2 ? 0 : 4 * (grp - 1)) + 2 * hh + ((idx >> 2) & 1);
    if (wid >= 4) {
        const char* xg0 = (isB ? xB : xA) + (size_t)rt * 128 * (DM * 2);
#pragma unroll
        for (int i = 0; i < 4; ++i) { const int row = (4 * (wid - 4) + i) * 8 + (lane >> 3); glds16(xg0, (unsigned)(row * (DM * 2) + (((lane & 7) ^ ((row >> 1) & 7)) << 4)), (unsigned)(uintptr_t)smem + K1_X + (4 * (wid - 4) + i) * 1024); }
    }
    __builtin_amdgcn_sched_barrier(0);
    STAMP_DECL;
    const int kind = isB ? 0 : ct >> 2, head0 = 8 * (ct & 3), row0 = rt * 128;
    char* dst = kind == 2 ? ws + VF_OFF + (size_t)head0 * VF_HEAD + (row0 >> 4) * VB : kind == 1 ? ws + KF_OFF + (size_t)head0 * KF_HEAD + (row0 >> 5) * KT : ws + QF_OFF + (size_t)((isB ? 32 : 0) + head0) * HEAD40 + row0 * 40;
    const unsigned dstride = kind == 2 ? VF_HEAD : kind == 1 ? KF_HEAD : HEAD40;
    if (wid >= 4) {
        const char* xg = (isB ? xB : xA) + (size_t)row0 * (DM * 2);
        const int wpitch = isB ? DM * 2 : 3 * DM * 2;
        const char* wg = (isB ? wq : wqkv) + ((ct * 320) & ~127);
        const char* w2g = ct >= 2 ? wv : wk;
        const unsigned lds0 = (unsigned)(uintptr_t)smem;
        const char* tdst = (kind == 0 || (row0 & 128) == 0) ? (const char*)((uintptr_t)dst & ~(uintptr_t)127) : nullptr;
        if (isB && wid < 7) k1_loader<11>(xg, wg, w2g, wpitch, wid - 4, lane, lds0, tdst, dstride); else k1_loader<10>(xg, wg, w2g, wpitch, wid - 4, lane, lds0, tdst, dstride);
        asm volatile("s_barrier" ::: "memory");
        { lds_ptr st = (lds_ptr)smem; const int t = tid - 256;
          if (kind == 1 && t < 32) *(__attribute__((address_space(3))) u32x4*)(st + t * KT + 1280) = u32x4{0u, 0u, 0u, 0u};
          if (kind == 2) {
              if (t < 64) *(__attribute__((address_space(3))) u32x4*)(st + t * VB + 672) = u32x4{0u, 0u, 0u, 0u};
              if (t >= 64 && t < 192) *(__attribute__((address_space(3))) u32x4*)(st + ((t - 64) >> 1) * VB + (t & 1) * 336 + 320) = u32x4{0x3F803F80u, 0x3F803F80u, 0x3F803F80u, 0x3F803F80u};
          } }
        BARRIER();
    } else {
        const char* ahead;
        {
            const int bb = blockIdx.x, j0 = lane & 31, j = (j0 < 27 || bb < 8) ? j0 : 26, s = 2 + 2 * wid + (lane >> 5);
            if (j < 16) ahead = (j < 8 ? xA : xB) + (size_t)(8 * bb + (j & 7)) * (DM * 2) + s * 128;
            else if (j < 24) { int n = 8 * bb + j - 16; n = n < 1920 ? n : 1919; const int r = n / 30; ahead = wqkv + (size_t)(64 * s + r) * (3 * DM * 2) + (n - 30 * r) * 128; }
            else if (j < 27) { int n = 3 * bb + j - 24; n = n < 640 ? n : 639; const int r = n / 10; ahead = wq + (size_t)(64 * s + r) * (DM * 2) + (n - 10 * r) * 128; }
            else { const int n = 5 * bb + j - 27; ahead = (n < 20 ? wk + n * 128 : wv + (n - 20) * 128) + s * 2560; }
            touch_addr(ahead, rfl((unsigned)(uintptr_t)smem + 3 * K1_SLOT));
        }
        const int shift32 = (ct & 1) * 2, xo = (ct & 1) * 12;
        if (isB) { if (ct < 2) k1_consumer<0, 1>(ws, smem, wid, lane, shift32, xo, row0, ahead STP_ARG); else k1_consumer<0, 2>(ws, smem, wid, lane, shift32, xo, row0, ahead STP_ARG); }
        else if (kind == 2) k1_consumer<2, 0>(ws, smem, wid, lane, shift32, 0, row0, ahead STP_ARG);
        else if (kind == 1) k1_consumer<1, 0>(ws, smem, wid, lane, shift32, 0, row0, ahead STP_ARG);
        else k1_consumer<0, 0>(ws, smem, wid, lane, shift32, 0, row0, ahead STP_ARG);
    }
    if (kind == 2) k1_copy_out<8 * VB>(smem, dst, dstride, wid, lane);
    else if (kind == 1) k1_copy_out<4 * KT>(smem, dst, dstride, wid, lane);
    else k1_copy_out<5120>(smem, dst, dstride, wid, lane);
    if (wid < 4) { STAMP(1, 5); DIAG_DRAIN(); STAMP(1, 7); STAMP_DUMP(1); }
}

extern "C" void kernel_launch(void* const* d_in, const int* in_sizes, int n_in, void* d_out, int out_size, void* d_ws, size_t ws_size, hipStream_t stream) {
    (void)in_sizes; (void)n_in; (void)out_size; (void)ws_size;
    const char* xA = (const char*)d_in[0]; const char* wqkv = (const char*)d_in[1]; const char* woA = (const char*)d_in[2];
    const char* xB = (const char*)d_in[3]; const char* wq = (const char*)d_in[4]; const char* wk = (const char*)d_in[5]; const char* wv = (const char*)d_in[6]; const char* woB = (const char*)d_in[7];
    char* ws = (char*)d_ws;
    (void)hipFuncSetAttribute((const void*)proj_kernel, hipFuncAttributeMaxDynamicSharedMemorySize, K1_LDS);
    (void)hipFuncSetAttribute((const void*)attn_kernel, hipFuncAttributeMaxDynamicSharedMemorySize, A_LDS);
    (void)hipFuncSetAttribute((const void*)outproj_kernel, hipFuncAttributeMaxDynamicSharedMemorySize, K3_LDS + 256);
    proj_kernel<<<256, 512, K1_LDS, stream>>>(xA, wqkv, xB, wq, wk, wv, ws);
    attn_kernel<<<256, 1024, A_LDS, stream>>>(ws, woA, woB);
    outproj_kernel<<<240, 512, K3_LDS + 256, stream>>>(ws, woA, woB, (char*)d_out);
}
```

```cpp
#include <hip/hip_runtime.h>
#include <stdint.h>

typedef short bf16x8 __attribute__((ext_vector_type(8)));
typedef short bf16x4 __attribute__((ext_vector_type(4)));
typedef float f32x2 __attribute__((ext_vector_type(2)));
typedef float f32x4 __attribute__((ext_vector_type(4)));
typedef float f32x16 __attribute__((ext_vector_type(16)));
typedef unsigned u32x2 __attribute__((ext_vector_type(2)));
typedef unsigned u32x4 __attribute__((ext_vector_type(4)));
typedef __bf16 bf16x2_t __attribute__((ext_vector_type(2)));
typedef short v4i16_t __attribute__((ext_vector_type(4)));
typedef __attribute__((address_space(3))) const char* lds_cptr;
typedef __attribute__((address_space(3))) char* lds_ptr;

#define DI __device__ __forceinline__

constexpr int SEQ = 2048, DM = 640;
constexpr float QSCALE = 0.22360679774997896f * 1.4426950408889634f;

constexpr unsigned HEAD40 = SEQ * 40, KT = 1296, VB = 688, KF_HEAD = 64 * KT, VF_HEAD = 128 * VB;
constexpr unsigned QF_OFF = 0, KF_OFF = QF_OFF + 64 * HEAD40, VF_OFF = KF_OFF + 33 * KF_HEAD, OF_OFF = VF_OFF + 33 * VF_HEAD + 2048;

DI unsigned cvtpk(float lo, float hi) { f32x2 v = {lo, hi}; bf16x2_t b = __builtin_convertvector(v, bf16x2_t); return __builtin_bit_cast(unsigned, b); }
DI u32x2 pack4(f32x4 a) { return u32x2{cvtpk(a[0], a[1]), cvtpk(a[2], a[3])}; }
DI unsigned rfl(unsigned x) { return __builtin_amdgcn_readfirstlane(x); }
DI void glds16(const char* sbase, unsigned voff, unsigned lds_dst) {
    asm volatile("s_mov_b32 m0, %2\n\ts_nop 0\n\tglobal_load_lds_dwordx4 %0, %1" :: "v"(voff), "s"(sbase), "s"(lds_dst) : "memory");
}
template <int N> DI void wait_vm() { asm volatile("s_waitcnt vmcnt(%0)" :: "n"(N) : "memory"); }
DI bf16x8 lds_b128(lds_cptr p) { return *(const __attribute__((address_space(3))) bf16x8*)p; }
DI bf16x4 lds_b64(lds_cptr p) { return *(const volatile __attribute__((address_space(3))) bf16x4*)p; }
DI bf16x4 lds_tr(lds_cptr p) { return __builtin_bit_cast(bf16x4, __builtin_amdgcn_ds_read_tr16_b64_v4i16((__attribute__((address_space(3))) v4i16_t*)p)); }
DI void lds_st64(lds_ptr p, u32x2 v) { *(__attribute__((address_space(3))) u32x2*)p = v; }
DI bf16x8 cat8(bf16x4 a, bf16x4 b) { return __builtin_shufflevector(a, b, 0, 1, 2, 3, 4, 5, 6, 7); }
DI int swap23(int r) { return (r & ~12) | ((r & 4) << 1) | ((r & 8) >> 1); }
DI void touch_lines(const char* p, unsigned voff, unsigned lds_scratch) {
    asm volatile("s_mov_b32 m0, %2\n\ts_nop 0\n\tglobal_load_lds_dword %0, %1" :: "v"(voff), "s"(p), "s"(lds_scratch) : "memory");
}
DI void touch_addr(const char* p, unsigned lds_scratch) {
    asm volatile("s_mov_b32 m0, %1\n\ts_nop 0\n\tglobal_load_lds_dword %0, off" :: "v"(p), "s"(lds_scratch) : "memory");
}
#define CODE_ADDR(var, sym) asm volatile("s_getpc_b64 s[60:61]\n\ts_add_u32 s60, s60, " sym "@rel32@lo+4\n\ts_addc_u32 s61, s61, " sym "@rel32@hi+12\n\ts_mov_b64 %0, s[60:61]" : "=s"(var) :: "scc", "s60", "s61")
template <int POL> DI void st128(char* p, bf16x8 v) {
    if (POL == 0) *(bf16x8*)p = v;
    else if (POL == 1) asm volatile("global_store_dwordx4 %0, %1, off nt\n\ts_nop 1" :: "v"(p), "v"(v) : "memory");
    else if (POL == 2) asm volatile("global_store_dwordx4 %0, %1, off sc1\n\ts_nop 1" :: "v"(p), "v"(v) : "memory");
    else asm volatile("global_store_dwordx4 %0, %1, off sc0 sc1\n\ts_nop 1" :: "v"(p), "v"(v) : "memory");
}
#define MFMA16(a, b, c) __builtin_amdgcn_mfma_f32_16x16x32_bf16(a, b, c, 0, 0, 0)
#define BARRIER() do { asm volatile("" ::: "memory"); __builtin_amdgcn_s_waitcnt(0xC07F); __builtin_amdgcn_s_barrier(); asm volatile("" ::: "memory"); } while (0)
#define SBAR() __builtin_amdgcn_sched_barrier(0)
template <int NM, int NR> DI void interleave() {
#pragma unroll
    for (int i = 0; i < NR / 1; ++i) { __builtin_amdgcn_sched_group_barrier(0x008, 1, 0); __builtin_amdgcn_sched_group_barrier(0x100, 1, 0); }
    __builtin_amdgcn_sched_group_barrier(0x008, NM - NR / 1, 0);
    SBAR();
}

constexpr int K3_X = 0, K3_W = 32 * 3840, K3_LDS = K3_W + 4 * 8192;
constexpr int k3_total(int t) { int n = 14; for (int u = 0; u < t; ++u) n += (u <= 16 ? 2 : 0) + (u <= 10 ? 2 : 0); return n; }
constexpr int k3_allowed(int t) {
    int last = t <= 2 ? 10 + 2 * t : k3_total(t - 3) + 2;
    const int xg = t == 5 ? 3 : t == 10 ? 7 : t == 15 ? 10 : -1;
    if (xg >= 0) { const int e = k3_total(xg) + 4; if (e > last) last = e; }
    return k3_total(t) - last;
}
DI int k3_krow(int c, int e) { const int P = c / 5, r = c - 5 * P; return r < 4 ? (2 * P + (r >> 1)) * 20 + (r & 1) * 8 + e : (2 * P + (e >> 2)) * 20 + 16 + (e & 3); }

DI unsigned k3_xoff(int o, int rot) { const int hd = o / 3840; return (unsigned)(((hd + 8 * rot) & 31) * HEAD40 + (o - hd * 3840)); }
template <int T> DI void k3_load_steps(const char* xsrc, const char* wsrc, const unsigned (&wo)[5][2], int lw, int lane, unsigned lds0, int rot) {
    if constexpr (T < 20) {
        wait_vm<k3_allowed(T)>();
        asm volatile("s_barrier" ::: "memory");
        if constexpr (T + 3 < 20) {
            constexpr int t = T + 3;
#pragma unroll
            for (int i = 0; i < 2; ++i) glds16(wsrc + (size_t)((t / 5 + rot) & 3) * 160 * (DM * 2), wo[t % 5][i], lds0 + K3_W + (t & 3) * 8192 + (2 * lw + i) * 1024);
        }
        if constexpr (T <= 10) {
#pragma unroll
            for (int i = 0; i < 2; ++i) { const int pc = lw + 4 * (8 + 2 * T + i); glds16(xsrc, k3_xoff(pc * 1024 + lane * 16, rot), lds0 + K3_X + pc * 1024); }
        }
        k3_load_steps<T + 1>(xsrc, wsrc, wo, lw, lane, lds0, rot);
    }
}

extern "C" __global__ __launch_bounds__(512, 2) void outproj_kernel(char* ws, const char* woA, const char* woB, char* out) {
    extern __shared__ __attribute__((aligned(16))) char smem[];
    const int tid = threadIdx.x, lane = tid & 63, wid = (int)rfl(tid >> 6);
    const int xcd = blockIdx.x & 7, idx = blockIdx.x >> 3, br = xcd >> 2;
    const int blk = xcd & 3, rot = xcd >> 1;
    if (idx >= 30) return;
    const int rl = idx / 5, ct = idx - 5 * rl, row0 = blk * 512 + (rl < 5 ? rl * 96 : 416);
    if (__builtin_expect(wid >= 4, 1)) {
        const int lw = wid - 4;
        const char* xsrc = ws + OF_OFF + (size_t)br * 32 * HEAD40 + row0 * 40;
        const char* wsrc = (br ? woB : woA) + ct * 256;
        const unsigned lds0 = (unsigned)(uintptr_t)smem;
#pragma unroll
        for (int j = 0; j < 8; ++j) { const int pc = lw + 4 * j; glds16(xsrc, k3_xoff(pc * 1024 + lane * 16, rot), lds0 + K3_X + pc * 1024); }
        SBAR();
        unsigned wo[5][2];
#pragma unroll
        for (int tt = 0; tt < 5; ++tt)
#pragma unroll
            for (int i = 0; i < 2; ++i) {
                const int rho = 4 * (2 * lw + i) + (lane >> 4), s16 = lane & 15, kap = swap23(rho);
                wo[tt][i] = (unsigned)(k3_krow(4 * tt + (kap >> 3), kap & 7) * (DM * 2) + (((s16 >> 1) ^ (rho & 7)) << 5) + (s16 & 1) * 16);
            }
#pragma unroll
        for (int t = 0; t < 3; ++t)
#pragma unroll
            for (int i = 0; i < 2; ++i) glds16(wsrc + (size_t)rot * 160 * (DM * 2), wo[t][i], lds0 + K3_W + t * 8192 + (2 * lw + i) * 1024);
        k3_load_steps<0>(xsrc, wsrc, wo, lw, lane, lds0, rot);
        asm volatile("s_barrier\n\ts_barrier" ::: "memory");
    } else {
        lds_cptr lds = (lds_cptr)smem;
        const int wm = wid >> 1, wn = wid & 1, m = lane & 15, g = lane >> 4, q = (lane & 15) >> 2, p = lane & 3;
        unsigned xlo[5], xhi[5], wa[4];
#pragma unroll
        for (int tt = 0; tt < 5; ++tt) {
            const int c = 4 * tt + g, P = c / 5, r = c - 5 * P, rb = (48 * wm + m) * 40;
            xlo[tt] = (unsigned)(K3_X + rb + (r < 4 ? (2 * P + (r >> 1)) * 3840 + (r & 1) * 16 : 2 * P * 3840 + 32));
            xhi[tt] = (unsigned)(K3_X + rb + (r < 4 ? (2 * P + (r >> 1)) * 3840 + (r & 1) * 16 + 8 : (2 * P + 1) * 3840 + 32));
        }
        { const int sx = (g & 1) * 4 + q;
#pragma unroll
          for (int j = 0; j < 4; ++j) wa[j] = (unsigned)(K3_W + ((g >> 1) * 16 + sx) * 256 + (((4 * wn + j) ^ sx) << 5) + p * 8); }
        f32x4 acc[3][4];
#pragma unroll
        for (int i = 0; i < 3; ++i)
#pragma unroll
            for (int j = 0; j < 4; ++j) acc[i][j] = f32x4{0.f, 0.f, 0.f, 0.f};
        bf16x8 xf[2][3], wf[2][4];
        auto load = [&](int t) {
            const int tt = t % 5, u = t / 5;
#pragma unroll
            for (int i = 0; i < 3; ++i) xf[t & 1][i] = cat8(lds_b64(lds + xlo[tt] + u * 30720 + i * 640), lds_b64(lds + xhi[tt] + u * 30720 + i * 640));
#pragma unroll
            for (int j = 0; j < 4; ++j) wf[t & 1][j] = cat8(lds_tr(lds + wa[j] + (t & 3) * 8192), lds_tr(lds + wa[j] + (t & 3) * 8192 + 2048));
        };
        BARRIER();
        load(0);
        SBAR();
#pragma unroll
        for (int t = 0; t < 20; ++t) {
            if (t + 1 < 20) { BARRIER(); load(t + 1); }
#pragma unroll
            for (int i = 0; i < 3; ++i)
#pragma unroll
                for (int j = 0; j < 4; ++j) acc[i][j] = MFMA16(wf[t & 1][j], xf[t & 1][i], acc[i][j]);
            if (t + 1 < 20) {
#pragma unroll
                for (int i = 0; i < 14 / 2; ++i) { __builtin_amdgcn_sched_group_barrier(0x100, 2, 0); __builtin_amdgcn_sched_group_barrier(0x008, 1, 0); }
                __builtin_amdgcn_sched_group_barrier(0x100, 14, 0); __builtin_amdgcn_sched_group_barrier(0x008, 12, 0);
            }
            SBAR();
        }
        BARRIER();
        lds_ptr st = (lds_ptr)smem;
#pragma unroll
        for (int i = 0; i < 3; ++i) {
            const int row = 48 * wm + 16 * i + m;
#pragma unroll
            for (int j = 0; j < 4; ++j) { const int b = (64 * wn + 16 * j + 4 * g) * 2; lds_st64(st + row * 256 + ((((b >> 4) ^ (row & 15)) << 4) | (b & 8)), pack4(acc[i][j])); }
        }
        BARRIER();
    }
#pragma unroll
    for (int it = 0; it < 3; ++it) {
        const int o = it * 8192 + tid * 16, row = o >> 8, s = (o >> 4) & 15;
        st128<2>(out + (size_t)(row0 + row) * 2560 + br * 1280 + ct * 256 + ((s ^ (row & 15)) << 4), lds_b128((lds_cptr)smem + o));
    }
}

constexpr int NSTAGE = DM / 64;
constexpr int K1_X = 0, K1_W = 16384, K1_W2 = K1_W + 24576, K1_SLOT = K1_W2 + 3072, K1_LDS = 3 * K1_SLOT + 256;

template <int NP> DI void k1_loader(const char* xg, const char* wg, const char* w2g, int wpitch, int lw, int lane, unsigned lds0) {
    unsigned xo[4], wo[6], w2o = 0;
#pragma unroll
    for (int i = 0; i < 4; ++i) { const int row = (4 * lw + i) * 8 + (lane >> 3), pos = lane & 7; xo[i] = (unsigned)(row * (DM * 2) + ((pos ^ ((row >> 1) & 7)) << 4)); }
#pragma unroll
    for (int j = 0; j < 3; ++j) {
        const int o = j * 1024 + lane * 16, r = (o * 171) >> 16, b = o - r * 384;
        wo[j] = (unsigned)((16 * lw + 8 * (r >> 2) + (r & 3)) * wpitch + (((b >> 5) ^ ((r >> 1) & 3)) << 5) + (b & 16));
        wo[j + 3] = wo[j] + 4u * (unsigned)wpitch;
    }
    const int p2 = lw;
    if (NP == 11) { const int o = p2 * 1024 + lane * 16; w2o = (unsigned)(o < 2560 ? o : 2544); }
    auto issue_w = [&](int s, int slot) {
        const unsigned sl = lds0 + (unsigned)slot * K1_SLOT;
        const char* wsrc = wg + (size_t)s * 64 * wpitch;
#pragma unroll
        for (int i = 0; i < 6; ++i) glds16(wsrc, wo[i], sl + K1_W + (6 * lw + i) * 1024);
        if (NP == 11) glds16(w2g + (size_t)s * 2560, w2o, sl + K1_W2 + p2 * 1024);
    };
    auto issue = [&](int s, int slot) {
        const unsigned sl = lds0 + (unsigned)slot * K1_SLOT;
        const char* xs = xg + (size_t)s * 128;
#pragma unroll
        for (int i = 0; i < 4; ++i) glds16(xs, xo[i], sl + K1_X + (4 * lw + i) * 1024);
        issue_w(s, slot);
    };
    issue_w(0, 0); issue(1, 1);
    int nslot = 2;
#pragma unroll 1
    for (int s = 0; s < NSTAGE; ++s) {
        if (s < NSTAGE - 1) wait_vm<NP>(); else wait_vm<0>();
        asm volatile("s_barrier" ::: "memory");
        if (s + 2 < NSTAGE) { issue(s + 2, nslot); nslot = nslot == 2 ? 0 : nslot + 1; }
    }
}

struct K1Frag { bf16x8 x[4]; bf16x8 w[5]; bf16x8 w2; };

template <int KIND, int XTRA> DI void k1_consumer(char* ws, char* smem, int wid, int lane, int shift32, int xo, int row0) {
    lds_cptr lds = (lds_cptr)smem;
    const int wm = wid >> 1, wn = wid & 1, m = lane & 15, g = lane >> 4, q = (lane & 15) >> 2, p = lane & 3;
    const unsigned xa0 = K1_X + (unsigned)((64 * wm + m) * 128 + ((g ^ ((m >> 1) & 7)) << 4));
    unsigned wa[5];
    { const int gx = ((g & 1) << 1) | (q >> 1);
#pragma unroll
      for (int j = 0; j < 5; ++j) wa[j] = K1_W + (unsigned)(((g >> 1) * 16 + (g & 1) * 4 + q) * 384 + (((shift32 + 5 * wn + j) ^ gx) << 5) + p * 8); }
    const unsigned w2a = K1_W2 + (unsigned)((8 * g + q) * 40 + p * 8 + xo * 2);
    f32x4 acc[4][5], acc2[2];
#pragma unroll
    for (int i = 0; i < 4; ++i)
#pragma unroll
        for (int j = 0; j < 5; ++j) acc[i][j] = f32x4{0.f, 0.f, 0.f, 0.f};
    acc2[0] = acc2[1] = f32x4{0.f, 0.f, 0.f, 0.f};
    auto load = [&](K1Frag& f, unsigned sl, int kk) {
        lds_cptr b = lds + sl;
#pragma unroll
        for (int i = 0; i < 4; ++i) f.x[i] = lds_b128(b + ((xa0 ^ (kk * 64)) + i * 2048));
#pragma unroll
        for (int j = 0; j < 5; ++j) f.w[j] = cat8(lds_tr(b + wa[j] + kk * 12288), lds_tr(b + wa[j] + kk * 12288 + 3072));
        if (XTRA) f.w2 = cat8(lds_tr(b + w2a + kk * 1280), lds_tr(b + w2a + kk * 1280 + 160));
    };
    auto mma = [&](const K1Frag& f) {
#pragma unroll
        for (int i = 0; i < 4; ++i)
#pragma unroll
            for (int j = 0; j < 5; ++j) acc[i][j] = KIND == 2 ? MFMA16(f.x[i], f.w[j], acc[i][j]) : MFMA16(f.w[j], f.x[i], acc[i][j]);
        if (XTRA) {
#pragma unroll
            for (int i = 0; i < 2; ++i) { const bf16x8 xx = wn ? f.x[2 + i] : f.x[i]; acc2[i] = XTRA == 2 ? MFMA16(xx, f.w2, acc2[i]) : MFMA16(f.w2, xx, acc2[i]); }
        }
    };
    constexpr int CH = KIND == 0 ? 5120 : KIND == 1 ? 4 * KT : 8 * VB, TP = KIND == 0 ? 1280 : KT;
    unsigned sa[5], sh[5];
#pragma unroll
    for (int j = 0; j < 5; ++j) {
        const int col = 80 * wn + 16 * j + (KIND == 2 ? m : 4 * g), hl = (col * 205) >> 12, d = col - hl * 20;
        if (KIND == 2) { sa[j] = (unsigned)(hl * CH + 4 * wm * VB + (g & 1) * 336 + (g >> 1) * 8 + d * 16); sh[j] = 0; }
        else { const bool rem = d == 16; sa[j] = (unsigned)(hl * CH + 2 * wm * TP + (rem ? 1024 + m * 8 : (d >> 3) * 512 + m * 16 + (d & 4) * 2)); sh[j] = sa[j] + (rem ? 128 : 256); asm volatile("" : "+v"(sh[j])); }
        asm volatile("" : "+v"(sa[j]));
    }
    K1Frag f0, f1;
    BARRIER();
    const unsigned scratch = rfl((unsigned)(uintptr_t)smem + 3 * K1_SLOT);
    load(f0, 0, 0);
    int slot = 0;
    constexpr int NM = XTRA ? 22 : 20, NR = XTRA ? 16 : 14;
    SBAR();
#pragma unroll 1
    for (int s = 0; s < NSTAGE - 1; ++s) {
        load(f1, (unsigned)slot * K1_SLOT, 1);
        mma(f0);
        interleave<NM, NR>();
        slot = slot == 2 ? 0 : slot + 1;
        BARRIER();
        if (wid == (s & 3)) {
            if (s == 8) { const char* code; CODE_ADDR(code, "attn_kernel"); touch_lines(code, (lane < 36 ? lane : 35) * 128, scratch); }
            if (s == 7) { const char* kd; CODE_ADDR(kd, "outproj_kernel.kd"); touch_lines(kd, (lane & 1) * 128, scratch); }
            SBAR();
        }
        load(f0, (unsigned)slot * K1_SLOT, 0);
        mma(f1);
        interleave<NM, NR>();
    }
    load(f1, (unsigned)slot * K1_SLOT, 1);
    mma(f0);
    interleave<NM, NR>();
    mma(f1);
    BARRIER();
    lds_ptr st = (lds_ptr)smem;
#pragma unroll
    for (int j = 0; j < 5; ++j) {
        if (KIND == 2) {
#pragma unroll
            for (int i = 0; i < 4; ++i) lds_st64(st + sa[j] + i * VB, pack4(acc[i][j]));
        } else {
            const float sc = KIND == 0 ? QSCALE : 1.0f;
            lds_st64(st + sa[j], pack4(acc[0][j] * sc)); lds_st64(st + sa[j] + TP, pack4(acc[2][j] * sc));
            lds_st64(st + sh[j], pack4(acc[1][j] * sc)); lds_st64(st + sh[j] + TP, pack4(acc[3][j] * sc));
        }
    }
    if (XTRA == 1) {
        const int d0 = xo + 4 * g;
        if (d0 < (xo ? 20 : 12)) {
#pragma unroll
            for (int i = 0; i < 2; ++i) {
                const int row = row0 + 64 * wm + 32 * wn + 16 * i + m, key = row & 31;
                *(u32x2*)(ws + KF_OFF + (size_t)32 * KF_HEAD + (row >> 5) * KT + (d0 < 16 ? (d0 >> 3) * 512 + key * 16 + (d0 & 4) * 2 : 1024 + key * 8)) = pack4(acc2[i]);
            }
        }
        if (xo == 0 && lane == 0 && wn == 0) {
#pragma unroll
            for (int i = 0; i < 2; ++i) *(u32x4*)(ws + KF_OFF + (size_t)32 * KF_HEAD + ((row0 >> 5) + 2 * wm + i) * KT + 1280) = u32x4{0u, 0u, 0u, 0u};
        }
    }
    if (XTRA == 2) {
        const int d = xo + m;
#pragma unroll
        for (int i = 0; i < 2; ++i) {
            char* dst = ws + VF_OFF + (size_t)32 * VF_HEAD + ((row0 >> 4) + 4 * wm + 2 * wn + i) * VB;
            if (d < (xo ? 20 : 12)) *(u32x2*)(dst + (g & 1) * 336 + (g >> 1) * 8 + d * 16) = pack4(acc2[i]);
            if (xo == 0 && m == 15) *(u32x2*)(dst + (g & 1) * 336 + (g >> 1) * 8 + 320) = u32x2{0x3F803F80u, 0x3F803F80u};
            if (xo == 0 && lane == 14) *(u32x4*)(dst + 672) = u32x4{0u, 0u, 0u, 0u};
        }
    }
    BARRIER();
}

template <int CH> DI void k1_copy_out(const char* smem, char* dst, unsigned stride, int wid, int lane) {
    lds_cptr src = (lds_cptr)smem + wid * CH + lane * 16;
    char* d = dst + (size_t)wid * stride + lane * 16;
    bf16x8 v[5];
#pragma unroll
    for (int i = 0; i < 5; ++i) v[i] = lds_b128(src + i * 1024);
#pragma unroll
    for (int i = 0; i < 5; ++i) st128<2>(d + i * 1024, v[i]);
    if (CH > 5120 && lane < (CH - 5120) / 16) st128<2>(d + 5120, lds_b128(src + 5120));
}

extern "C" __global__ __launch_bounds__(512, 2) void proj_kernel(const char* xA, const char* wqkv, const char* xB, const char* wq, const char* wk, const char* wv, char* ws) {
    extern __shared__ __attribute__((aligned(16))) char smem[];
    const int tid = threadIdx.x, lane = tid & 63, wid = (int)rfl(tid >> 6);
    {
        const char* code; CODE_ADDR(code, "proj_kernel");
        if (wid < 3) touch_lines(code + wid * 8192, lane * 128, rfl((unsigned)(uintptr_t)smem + 3 * K1_SLOT));
        __builtin_amdgcn_sched_barrier(0);
    }
    const int xcd = blockIdx.x & 7, idx = blockIdx.x >> 3, blk = xcd & 3, hh = xcd >> 2, grp = idx >> 3;
    const bool isB = grp == 1;
    const int rt = 4 * blk + (idx & 3);
    const int ct = (grp < 2 ? 0 : 4 * (grp - 1)) + 2 * hh + ((idx >> 2) & 1);
    if (wid >= 4) {
        const char* xg0 = (isB ? xB : xA) + (size_t)rt * 128 * (DM * 2);
#pragma unroll
        for (int i = 0; i < 4; ++i) { const int row = (4 * (wid - 4) + i) * 8 + (lane >> 3); glds16(xg0, (unsigned)(row * (DM * 2) + (((lane & 7) ^ ((row >> 1) & 7)) << 4)), (unsigned)(uintptr_t)smem + K1_X + (4 * (wid - 4) + i) * 1024); }
    }
    __builtin_amdgcn_sched_barrier(0);
    const int kind = isB ? 0 : ct >> 2, head0 = 8 * (ct & 3), row0 = rt * 128;
    char* dst = kind == 2 ? ws + VF_OFF + (size_t)head0 * VF_HEAD + (row0 >> 4) * VB : kind == 1 ? ws + KF_OFF + (size_t)head0 * KF_HEAD + (row0 >> 5) * KT : ws + QF_OFF + (size_t)((isB ? 32 : 0) + head0) * HEAD40 + row0 * 40;
    const unsigned dstride = kind == 2 ? VF_HEAD : kind == 1 ? KF_HEAD : HEAD40;
    if (__builtin_expect(wid >= 4, 1)) {
        const char* xg = (isB ? xB : xA) + (size_t)row0 * (DM * 2);
        const int wpitch = isB ? DM * 2 : 3 * DM * 2;
        const char* wg = (isB ? wq : wqkv) + ((ct * 320) & ~127);
        const char* w2g = ct >= 2 ? wv : wk;
        const unsigned lds0 = (unsigned)(uintptr_t)smem;
        if (isB && wid < 7) k1_loader<11>(xg, wg, w2g, wpitch, wid - 4, lane, lds0); else k1_loader<10>(xg, wg, w2g, wpitch, wid - 4, lane, lds0);
        asm volatile("s_barrier" ::: "memory");
        { lds_ptr st = (lds_ptr)smem; const int t = tid - 256;
          if (kind == 1 && t < 32) *(__attribute__((address_space(3))) u32x4*)(st + t * KT + 1280) = u32x4{0u, 0u, 0u, 0u};
          if (kind == 2) {
              if (t < 64) *(__attribute__((address_space(3))) u32x4*)(st + t * VB + 672) = u32x4{0u, 0u, 0u, 0u};
              if (t >= 64 && t < 192) *(__attribute__((address_space(3))) u32x4*)(st + ((t - 64) >> 1) * VB + (t & 1) * 336 + 320) = u32x4{0x3F803F80u, 0x3F803F80u, 0x3F803F80u, 0x3F803F80u};
          } }
        BARRIER();
    } else {
        const char* ahead;
        {
            const int bb = blockIdx.x, j0 = lane & 31, j = (j0 < 27 || bb < 8) ? j0 : 26, s = 2 + 2 * wid + (lane >> 5);
            if (j < 16) ahead = (j < 8 ? xA : xB) + (size_t)(8 * bb + (j & 7)) * (DM * 2) + s * 128;
            else if (j < 24) { int n = 8 * bb + j - 16; n = n < 1920 ? n : 1919; const int r = n / 30; ahead = wqkv + (size_t)(64 * s + r) * (3 * DM * 2) + (n - 30 * r) * 128; }
            else if (j < 27) { int n = 3 * bb + j - 24; n = n < 640 ? n : 639; const int r = n / 10; ahead = wq + (size_t)(64 * s + r) * (DM * 2) + (n - 10 * r) * 128; }
            else { const int n = 5 * bb + j - 27; ahead = (n < 20 ? wk + n * 128 : wv + (n - 20) * 128) + s * 2560; }
            touch_addr(ahead, rfl((unsigned)(uintptr_t)smem + 3 * K1_SLOT));
        }
        const int shift32 = (ct & 1) * 2, xo = (ct & 1) * 12;
        if (isB) { if (ct < 2) k1_consumer<0, 1>(ws, smem, wid, lane, shift32, xo, row0); else k1_consumer<0, 2>(ws, smem, wid, lane, shift32, xo, row0); }
        else if (kind == 2) k1_consumer<2, 0>(ws, smem, wid, lane, shift32, 0, row0);
        else if (kind == 1) k1_consumer<1, 0>(ws, smem, wid, lane, shift32, 0, row0);
        else k1_consumer<0, 0>(ws, smem, wid, lane, shift32, 0, row0);
    }
    if (kind == 2) k1_copy_out<8 * VB>(smem, dst, dstride, wid, lane);
    else if (kind == 1) k1_copy_out<4 * KT>(smem, dst, dstride, wid, lane);
    else k1_copy_out<5120>(smem, dst, dstride, wid, lane);
}

constexpr int A_PART = 6144, A_SLOT = 4 * A_PART, A_X = 3 * A_SLOT, A_LDS = A_X + 16 * 3072;
static_assert(4 * KT <= A_PART && 8 * VB <= A_PART && A_SLOT == 24576, "stage layout");

#define MFMA32(a, b, c) __builtin_amdgcn_mfma_f32_32x32x16_bf16(a, b, c, 0, 0, 0)
#define MFMA32R(a, b, c) __builtin_amdgcn_mfma_f32_32x32x8bf16_1k(a, b, c, 0, 0, 0)

DI void attn_exact(const char* kf, const char* vf, bf16x8 qm, bf16x4 qr, int lane, f32x16& o) {
    const int r32 = lane & 31, h = lane >> 5;
    float mrun = -3.0e38f;
#pragma unroll
    for (int r = 0; r < 16; ++r) o[r] = 0.f;
#pragma unroll 1
    for (int t = 0; t < 64; ++t) {
        const bf16x8 km = *(const bf16x8*)(kf + t * KT + lane * 16);
        const bf16x4 kr = *(const bf16x4*)(kf + t * KT + 1024 + r32 * 8);
        const int vo = r32 < 21 ? h * 336 + r32 * 16 : 672;
        const bf16x8 v0 = *(const bf16x8*)(vf + (2 * t) * VB + vo);
        const bf16x8 v1 = *(const bf16x8*)(vf + (2 * t + 1) * VB + vo);
        f32x16 s;
#pragma unroll
        for (int r = 0; r < 16; ++r) s[r] = 0.f;
        s = MFMA32(km, qm, s);
        s = MFMA32R(kr, qr, s);
        float mx = s[0];
#pragma unroll
        for (int r = 1; r < 16; ++r) mx = fmaxf(mx, s[r]);
        { auto rr = __builtin_amdgcn_permlane32_swap(__float_as_uint(mx), __float_as_uint(mx), false, false); mx = fmaxf(__uint_as_float(rr[0]), __uint_as_float(rr[1])); }
        const float mnew = fmaxf(mrun, mx), alpha = __builtin_amdgcn_exp2f(mrun - mnew);
        mrun = mnew;
        u32x4 p0, p1;
#pragma unroll
        for (int i = 0; i < 4; ++i) {
            p0[i] = cvtpk(__builtin_amdgcn_exp2f(s[2 * i] - mnew), __builtin_amdgcn_exp2f(s[2 * i + 1] - mnew));
            p1[i] = cvtpk(__builtin_amdgcn_exp2f(s[8 + 2 * i] - mnew), __builtin_amdgcn_exp2f(s[8 + 2 * i + 1] - mnew));
        }
#pragma unroll
        for (int r = 0; r < 16; ++r) o[r] *= alpha;
        o = MFMA32(v0, __builtin_bit_cast(bf16x8, p0), o);
        o = MFMA32(v1, __builtin_bit_cast(bf16x8, p1), o);
    }
}

DI float row_sum_of(const f32x16& o) {
    auto rr = __builtin_amdgcn_permlane32_swap(__float_as_uint(o[8]), __float_as_uint(o[8]), false, false);
    return __uint_as_float(rr[1]);
}

#define R_SA "v[64:79]"
#define R_SB "v[80:95]"
#define R_PL "v[100:103]"
#define R_PH "v[104:107]"
#define R_KM "v[108:111]"
#define R_KR "v[112:113]"
#define R_V0 "v[114:117]"
#define R_V1 "v[118:121]"
#define STR_(x) #x
#define STR(x) STR_(x)
#define MF16(d, a, b, c) "v_mfma_f32_32x32x16_bf16 " d ", " a ", " b ", " c "\n\t"
#define MF8(d, a, b, c) "v_mfma_f32_32x32x8_bf16 " d ", " a ", " b ", " c "\n\t"
#define CVT2(p0, p1) "v_cvt_pk_bf16_f32 v" STR(p0) ", v96, v97\n\tv_cvt_pk_bf16_f32 v" STR(p1) ", v98, v99\n\t"
#define EXP4(s0, s1, s2, s3) "v_exp_f32 v96, v" STR(s0) "\n\tv_exp_f32 v97, v" STR(s1) "\n\tv_exp_f32 v98, v" STR(s2) "\n\tv_exp_f32 v99, v" STR(s3) "\n\t"
#define GAP(p0, p1, s0, s1, s2, s3) CVT2(p0, p1) EXP4(s0, s1, s2, s3)
#define WAITL(n) "s_waitcnt lgkmcnt(" STR(n) ")\n\t"
#define RD128(d, a, off) "ds_read_b128 " d ", " a " offset:" STR(off) "\n\t"
#define RD64(d, a, off) "ds_read_b64 " d ", " a " offset:" STR(off) "\n\t"
#define TILE(MID, KOFF, V1OFF, V0OFF) \
    MF16(R_SB, R_KM, "%[qbm]", "0") RD128(R_KM, "v45", KOFF) GAP(106, 107, 64, 65, 66, 67) \
    MF8(R_SB, R_KR, "%[qbr]", R_SB) RD64(R_KR, "v46", KOFF) GAP(100, 101, 68, 69, 70, 71) \
    MF16("%[ob]", R_V1, R_PH, "%[ob]") RD128(R_V1, "v47", V1OFF) GAP(102, 103, 72, 73, 74, 75) \
    WAITL(3) MF16("%[oa]", R_V0, R_PL, "%[oa]") GAP(104, 105, 76, 77, 78, 79) \
    MID \
    WAITL(2) MF16(R_SA, R_KM, "%[qam]", "0") GAP(106, 107, 80, 81, 82, 83) \
    WAITL(1) MF8(R_SA, R_KR, "%[qar]", R_SA) GAP(100, 101, 84, 85, 86, 87) \
    WAITL(0) MF16("%[oa]", R_V1, R_PH, "%[oa]") GAP(102, 103, 88, 89, 90, 91) \
    MF16("%[ob]", R_V0, R_PL, "%[ob]") RD128(R_V0, "v47", V0OFF) GAP(104, 105, 92, 93, 94, 95)
#define TILE_LAST(MID, PRE_K, V1OFF, PRE_V0) \
    MF16(R_SB, R_KM, "%[qbm]", "0") PRE_K RD128(R_KM, "v45", 0) GAP(106, 107, 64, 65, 66, 67) \
    MF8(R_SB, R_KR, "%[qbr]", R_SB) RD64(R_KR, "v46", 0) GAP(100, 101, 68, 69, 70, 71) \
    MF16("%[ob]", R_V1, R_PH, "%[ob]") RD128(R_V1, "v47", V1OFF) GAP(102, 103, 72, 73, 74, 75) \
    WAITL(3) MF16("%[oa]", R_V0, R_PL, "%[oa]") GAP(104, 105, 76, 77, 78, 79) \
    MID \
    WAITL(2) MF16(R_SA, R_KM, "%[qam]", "0") GAP(106, 107, 80, 81, 82, 83) \
    WAITL(1) MF8(R_SA, R_KR, "%[qar]", R_SA) GAP(100, 101, 84, 85, 86, 87) \
    WAITL(0) MF16("%[oa]", R_V1, R_PH, "%[oa]") GAP(102, 103, 88, 89, 90, 91) \
    MF16("%[ob]", R_V0, R_PL, "%[ob]") PRE_V0 RD128(R_V0, "v47", 0) GAP(104, 105, 92, 93, 94, 95)

#define PRIO(n) "s_setprio " STR(n) "\n\t"
#define TOUCH_PF "global_load_lds_dword v42, %[pf]\n\t"
#define DMA2(SLOTREG) \
    "s_cmp_ge_u32 %[wid], 8\n\ts_cbranch_scc1 5f\n\t" \
    "s_lshr_b32 s46, s45, 1\n\ts_lshl_b32 s46, s46, 2\n\t" \
    "s_and_b32 s47, s45, 1\n\ts_add_u32 s46, s46, s47\n\ts_lshl_b32 s46, s46, 2\n\ts_add_u32 s45, s45, 1\n\t" \
    "s_mul_i32 s47, s46, 1296\n\ts_add_u32 s60, s48, s47\n\ts_addc_u32 s61, s49, 0\n\t" \
    "s_add_u32 m0, %[d0], " SLOTREG "\n\ts_mul_i32 s47, s46, %[p1]\n\tglobal_load_lds_dwordx4 v40, s[60:61]\n\t" \
    "s_add_u32 s60, s50, s47\n\ts_addc_u32 s61, s51, 0\n\t" \
    "s_add_u32 m0, m0, 0x2000\n\ts_mul_i32 s47, s46, 1376\n\tglobal_load_lds_dwordx4 v40, s[60:61]\n\t" \
    "s_add_u32 s60, s54, s47\n\ts_addc_u32 s61, s55, 0\n\t" \
    "s_add_u32 m0, m0, 0x2000\n\ts_nop 0\n\tglobal_load_lds_dwordx4 v40, s[60:61]\n\t" \
    "5:\n\t"

extern "C" __global__ __launch_bounds__(1024, 4) void attn_kernel(char* ws, const char* woA, const char* woB) {
    extern __shared__ __attribute__((aligned(16))) char smem[];
    const int tid = threadIdx.x, lane = tid & 63, wid = (int)rfl(tid >> 6);
    const int xcd = blockIdx.x & 7, idx = blockIdx.x >> 3;
    const int qblk = idx & 3, branch = idx >> 4, head = 4 * xcd + ((idx >> 2) & 3);
    const int kvh = branch ? 32 : head;
    const int qg = wid & 7, kh = wid >> 3;
    const unsigned kf_off = KF_OFF + kvh * KF_HEAD, vf_off = VF_OFF + kvh * VF_HEAD;
    const unsigned lds0 = (unsigned)(uintptr_t)smem;
    const unsigned w8 = wid & 7, w1k = w8 * 1024;
    const unsigned src0 = kf_off + w1k + (w8 >= 6 ? 8 * KT - 6144 : 0);
    const unsigned src1 = w8 < 4 ? kf_off + 8 * KT + 2048 + w1k : vf_off + w1k - 4096, pitch1 = w8 < 4 ? KT : 2 * VB;
    const unsigned src2 = vf_off + w1k + (w8 < 2 ? 4096 : 16 * VB - 2048);
    const unsigned d0 = lds0 + wid * 1024;
    const char* wo = xcd >> 2 ? woB : woA;
    asm volatile("" : "+s"(wo));
    if (wid < 8) {
        const unsigned l16 = lane * 16;
        glds16(ws + src0, l16, d0); glds16(ws + src1, l16, d0 + 8192); glds16(ws + src2, l16, d0 + 16384);
    }
    __builtin_amdgcn_sched_barrier(0);
    const int qrow0 = qblk * 512 + qg * 64;
    const unsigned qoff = rfl(QF_OFF + (unsigned)(branch * 32 + head) * HEAD40 + (unsigned)(qrow0 + 32 * kh) * 40), qoffb = rfl(QF_OFF + (unsigned)(branch * 32 + head) * HEAD40 + (unsigned)(qrow0 + 32 - 32 * kh) * 40);
    const unsigned kbase = rfl(lds0 + kh * A_PART), vbase = rfl(lds0 + 2 * A_PART + kh * A_PART);
    const char* pf; unsigned pfmax;
    { const char* code; CODE_ADDR(code, "outproj_kernel");
      pf = wid < 4 ? wo + (idx * 200 + wid * 50) * 128 : code + (wid == 5 ? 8192 : 0); pfmax = wid < 4 ? 49 : 63; }
    bf16x8 qam, qbm; bf16x4 qar, qbr;
    f32x16 oa, ob;
    asm volatile(

        "v_lshlrev_b32 v40, 4, %[lane]\n\t"
        "s_mov_b64 s[52:53], %[ws]\n\t"
        "s_add_u32 s48, s52, %[src0]\n\ts_addc_u32 s49, s53, 0\n\ts_add_u32 s50, s52, %[src1]\n\ts_addc_u32 s51, s53, 0\n\ts_add_u32 s54, s52, %[src2]\n\ts_addc_u32 s55, s53, 0\n\t"
        "s_mov_b32 s45, 1\n\t"
        "v_and_b32 v41, 31, %[lane]\n\tv_lshrrev_b32 v42, 5, %[lane]\n\t"
        "s_add_u32 s62, s52, %[qoff]\n\ts_addc_u32 s63, s53, 0\n\ts_add_u32 s56, s52, %[qoffb]\n\ts_addc_u32 s57, s53, 0\n\t"
        "v_lshlrev_b32 v43, 3, v41\n\t"
        "v_mov_b64 %[qar], 0\n\tv_mov_b64 %[qbr], 0\n\t"
        "global_load_dwordx4 %[qam], v40, s[62:63]\n\tglobal_load_dwordx4 %[qbm], v40, s[56:57]\n\t"
        "s_mov_b32 exec_hi, 0\n\t"
        "global_load_dwordx2 %[qar], v43, s[62:63] offset:1024\n\tglobal_load_dwordx2 %[qbr], v43, s[56:57] offset:1024\n\t"
        "s_mov_b32 exec_hi, -1\n\t"
        "v_cmp_eq_u32 vcc, 0, v42\n\t"
        "v_add_u32 v45, %[kbase], v40\n\t"
        "v_add_u32 v46, 0x400, v43\n\tv_mov_b32 v48, 0x500\n\ts_nop 0\n\t"
        "v_cndmask_b32 v46, v48, v46, vcc\n\tv_add_u32 v46, %[kbase], v46\n\t"
        "v_cmp_gt_u32 vcc, 21, v41\n\t"
        "v_mul_u32_u24 v47, 0x150, v42\n\tv_lshl_add_u32 v47, v41, 4, v47\n\tv_mov_b32 v48, 0x2a0\n\t"
        "v_cndmask_b32 v47, v48, v47, vcc\n\tv_add_u32 v47, %[vbase], v47\n\t"
        "v_mov_b32 v118, 0\n\tv_mov_b32 v119, 0\n\tv_mov_b32 v120, 0\n\tv_mov_b32 v121, 0\n\t"
        "v_mov_b32 v96, 0\n\tv_mov_b32 v97, 0\n\tv_mov_b32 v98, 0\n\tv_mov_b32 v99, 0\n\t"
        "v_mov_b32 v104, 0\n\tv_mov_b32 v105, 0\n\t"
        "v_mfma_f32_32x32x16_bf16 %[oa], v[118:121], v[118:121], 0\n\tv_mfma_f32_32x32x16_bf16 %[ob], v[118:121], v[118:121], 0\n\t"
        "s_mov_b32 s40, 0\n\t"
        "s_mov_b32 s41, 0\n\t"
        "s_mov_b32 s44, 24576\n\t"
        "s_waitcnt vmcnt(4)\n\t" "s_barrier\n\t"
        RD128(R_KM, "v45", 0) RD64(R_KR, "v46", 0) RD128(R_V0, "v47", 0)
        "s_waitcnt vmcnt(0)\n\t"
        DMA2("s44")
        WAITL(0)
        MF16(R_SA, R_KM, "%[qam]", "0") MF8(R_SA, R_KR, "%[qar]", R_SA)
        "s_nop 15\n\ts_nop 3\n\t"

        "2:\n\t"
        "s_add_u32 s42, s41, 24576\n\ts_cmp_eq_u32 s42, 73728\n\ts_cselect_b32 s42, 0, s42\n\t"
        "s_sub_u32 s43, s42, s41\n\t"
        "s_add_u32 s44, s42, 24576\n\ts_cmp_eq_u32 s44, 73728\n\ts_cselect_b32 s44, 0, s44\n\t"
        PRIO(2) TILE(PRIO(2), 1296, 688, 1376)
        PRIO(1) TILE(PRIO(1), 2592, 2064, 2752)
        PRIO(0) TILE(PRIO(0), 3888, 3440, 4128)
        PRIO(3) TILE_LAST(PRIO(3),
            "s_waitcnt vmcnt(0)\n\ts_barrier\n\t"
            "v_add_u32 v45, s43, v45\n\tv_add_u32 v46, s43, v46\n\t"
            "s_cmp_ge_u32 s40, 6\n\ts_cbranch_scc1 3f\n\t"
            DMA2("s44")
            "s_branch 4f\n\t"
            "3:\n\t"
            "s_cmp_lg_u32 s40, 6\n\ts_cbranch_scc1 4f\n\t"
            "v_lshrrev_b32 v42, 4, v40\n\tv_min_u32 v42, %[pfmax], v42\n\tv_lshlrev_b32 v42, 7, v42\n\t"
            "s_add_u32 m0, %[d0], s44\n\ts_nop 0\n\t"
            TOUCH_PF
            "4:\n\t",
            4816, "v_add_u32 v47, s43, v47\n\t")
        "s_mov_b32 s41, s42\n\t"
        "s_add_u32 s40, s40, 1\n\t"
        "s_cmp_lt_u32 s40, 8\n\ts_cbranch_scc1 2b\n\t"
        "s_nop 1\n\t" CVT2(106, 107) "s_nop 1\n\t"
        MF16("%[ob]", R_V1, R_PH, "%[ob]")
        "s_waitcnt lgkmcnt(0)\n\ts_nop 15\n\ts_nop 3\n\t"
        : [oa] "=&v"(oa), [ob] "=&v"(ob), [qam] "=&v"(qam), [qbm] "=&v"(qbm), [qar] "=&v"(qar), [qbr] "=&v"(qbr)
        : [lane] "v"(lane), [ws] "s"(ws), [src0] "s"(src0), [src1] "s"(src1), [src2] "s"(src2), [p1] "s"(pitch1), [qoff] "s"(qoff), [qoffb] "s"(qoffb),
          [kbase] "s"(kbase), [vbase] "s"(vbase), [d0] "s"(d0), [wid] "s"(wid), [pf] "s"(pf), [pfmax] "s"(pfmax)
        : "memory", "scc", "vcc", "s40", "s41", "s42", "s43", "s44", "s45", "s46", "s47", "s48", "s49", "s50", "s51", "s52", "s53", "s54", "s55", "s56", "s57", "s60", "s61", "s62", "s63",
          "s64", "s65", "s66", "s67", "s68", "s69", "s70", "s71", "s72", "s73",
          "v40", "v41", "v42", "v43", "v44", "v45", "v46", "v47", "v48",
          "v64", "v65", "v66", "v67", "v68", "v69", "v70", "v71", "v72", "v73", "v74", "v75", "v76", "v77", "v78", "v79",
          "v80", "v81", "v82", "v83", "v84", "v85", "v86", "v87", "v88", "v89", "v90", "v91", "v92", "v93", "v94", "v95",
          "v96", "v97", "v98", "v99", "v100", "v101", "v102", "v103", "v104", "v105", "v106", "v107", "v108", "v109", "v110", "v111",
          "v112", "v113", "v114", "v115", "v116", "v117", "v118", "v119", "v120", "v121");
    const int r32 = lane & 31, h = lane >> 5;
    {
        lds_ptr xb = (lds_ptr)smem + A_X + wid * 3072 + lane * 16;
#pragma unroll
        for (int i = 0; i < 3; ++i) *(__attribute__((address_space(3))) f32x4*)(xb + i * 1024) = f32x4{ob[4 * i], ob[4 * i + 1], ob[4 * i + 2], ob[4 * i + 3]};
    }
    f32x16 o = oa;
    BARRIER();
    {
        lds_cptr xb = (lds_cptr)smem + A_X + (wid ^ 8) * 3072 + lane * 16;
#pragma unroll
        for (int i = 0; i < 3; ++i) { const f32x4 v = *(const __attribute__((address_space(3))) f32x4*)(xb + i * 1024);
#pragma unroll
            for (int e = 0; e < 4; ++e) o[4 * i + e] += v[e]; }
    }
    float l = row_sum_of(o);
    const float mag = ((fabsf(o[0]) + fabsf(o[1])) + (fabsf(o[2]) + fabsf(o[3]))) + ((fabsf(o[4]) + fabsf(o[5])) + (fabsf(o[6]) + fabsf(o[7]))) + ((fabsf(o[8]) + fabsf(o[9])) + (fabsf(o[10]) + fabsf(o[11])));
    const bool ok = l >= 0x1p-60f && l <= 0x1p60f && mag <= 3.0e38f;
    if (__builtin_expect(!__all(ok), 0)) {
        attn_exact(ws + kf_off, ws + vf_off, qam, qar, lane, o);
        l = row_sum_of(o);
    }
    const float inv = __builtin_amdgcn_rcpf(l);
    {
        lds_ptr so = (lds_ptr)smem + A_X + (wid ^ 8) * 3072;
        lds_st64(so + r32 * 40 + 8 * h, u32x2{cvtpk(o[0] * inv, o[1] * inv), cvtpk(o[2] * inv, o[3] * inv)});
        lds_st64(so + r32 * 40 + 16 + 8 * h, u32x2{cvtpk(o[4] * inv, o[5] * inv), cvtpk(o[6] * inv, o[7] * inv)});
        if (h == 0) lds_st64(so + r32 * 40 + 32, u32x2{cvtpk(o[8] * inv, o[9] * inv), cvtpk(o[10] * inv, o[11] * inv)});
        asm volatile("" ::: "memory"); __builtin_amdgcn_s_waitcnt(0xC07F); asm volatile("" ::: "memory");
        char* od = ws + OF_OFF + (size_t)((unsigned)(branch * 32 + head) * HEAD40 + (unsigned)(qrow0 + 32 * kh) * 40) + lane * 16;
        st128<2>(od, lds_b128((lds_cptr)so + lane * 16));
        if (lane < 16) st128<2>(od + 1024, lds_b128((lds_cptr)so + 1024 + lane * 16));
    }
}

extern "C" void kernel_launch(void* const* d_in, const int* in_sizes, int n_in, void* d_out, int out_size, void* d_ws, size_t ws_size, hipStream_t stream) {
    (void)in_sizes; (void)n_in; (void)out_size; (void)ws_size;
    const char* xA = (const char*)d_in[0]; const char* wqkv = (const char*)d_in[1]; const char* woA = (const char*)d_in[2];
    const char* xB = (const char*)d_in[3]; const char* wq = (const char*)d_in[4]; const char* wk = (const char*)d_in[5]; const char* wv = (const char*)d_in[6]; const char* woB = (const char*)d_in[7];
    char* ws = (char*)d_ws;
    (void)hipFuncSetAttribute((const void*)proj_kernel, hipFuncAttributeMaxDynamicSharedMemorySize, K1_LDS);
    (void)hipFuncSetAttribute((const void*)attn_kernel, hipFuncAttributeMaxDynamicSharedMemorySize, A_LDS);
    (void)hipFuncSetAttribute((const void*)outproj_kernel, hipFuncAttributeMaxDynamicSharedMemorySize, K3_LDS);
    proj_kernel<<<256, 512, K1_LDS, stream>>>(xA, wqkv, xB, wq, wk, wv, ws);
    attn_kernel<<<256, 1024, A_LDS, stream>>>(ws, woA, woB);
    outproj_kernel<<<240, 512, K3_LDS, stream>>>(ws, woA, woB, (char*)d_out);
}
```
